```python
import torch

def _softmax(x, axis=-1):
    return torch.softmax(x, dim=axis)

def _gelu(x):
    return torch.nn.functional.gelu(x, approximate='tanh')

def _silu(x):
    return torch.nn.functional.silu(x)

def _sigmoid(x):
    return torch.sigmoid(x)

def _layernorm(x, axis=-1, eps=1e-05):
    m = x.mean(dim=axis, keepdim=True)
    v = x.var(dim=axis, keepdim=True, unbiased=False)
    return (x - m) / torch.sqrt(v + eps)

def _scatter_add(tgt, idx, src, axis=0):
    return tgt.clone().index_add_(axis, idx, src)

def _segment_sum(src, seg, n):
    out = torch.zeros((n,) + src.shape[1:], dtype=src.dtype)
    return out.index_add_(0, seg, src)

def _pad(x, pw):
    flat = tuple((v for pair in reversed(pw) for v in pair))
    return torch.nn.functional.pad(x, flat)
kernel_name = 'concat_heads__spectral_gfnet__memory_aug_recurrent_mem_tr__prompt'

def setup_inputs():

    _w = lambda a: (lambda t: t.to(torch.bfloat16) if t.is_floating_point() else t)(torch.as_tensor(a))

    def _A_setup():
        B, S, D = (1, 2048, 512)
        Df = D // 2 + 1
        return {'x': _w((lambda *s: torch.randn(*s, dtype=torch.bfloat16) * 0.05)(B, S, D)), 'w_r': _w((lambda *s: torch.randn(*s, dtype=torch.bfloat16) * 3.2)(S, Df) * 0.02), 'w_i': _w((lambda *s: torch.randn(*s, dtype=torch.bfloat16) * 3.2)(S, Df) * 0.02)}
    _A_in = _A_setup()

    def _B_setup():
        B, S, Dm = (1, 2048, 512)
        H, Dh = (32, 128)
        M, seg = (8, 32)
        seg = min(seg, S)
        S = S // seg * seg
        return {'x': _w((lambda *s: torch.randn(*s, dtype=torch.bfloat16) * 0.05)(B, S, Dm)), 'mem0': _w((lambda *s: torch.randn(*s, dtype=torch.bfloat16) * 0.4)(B, M, Dm)), 'Wq': _w((lambda *s: torch.randn(*s, dtype=torch.bfloat16) * 6.4)(Dm, H, Dh) * Dm ** (-0.5)), 'Wk': _w((lambda *s: torch.randn(*s, dtype=torch.bfloat16) * 6.4)(Dm, H, Dh) * Dm ** (-0.5)), 'Wv': _w((lambda *s: torch.randn(*s, dtype=torch.bfloat16) * 0.4)(Dm, H, Dh) * Dm ** (-0.5)), 'Wo': _w((lambda *s: torch.randn(*s, dtype=torch.bfloat16) * 0.4)(H, Dh, Dm) * (H * Dh) ** (-0.5)), 'seg_len': seg}
    _B_in = _B_setup()
    return {'A_x': _A_in['x'], 'A_w_r': _A_in['w_r'], 'A_w_i': _A_in['w_i'], 'B_x': _B_in['x'], 'B_mem0': _B_in['mem0'], 'B_Wq': _B_in['Wq'], 'B_Wk': _B_in['Wk'], 'B_Wv': _B_in['Wv'], 'B_Wo': _B_in['Wo'], 'B_seg_len': _B_in['seg_len']}

def reference(A_x, A_w_r, A_w_i, B_x, B_mem0, B_Wq, B_Wk, B_Wv, B_Wo, B_seg_len):

    def _A_ref(x, w_r, w_i):
        B, S, D = x.shape
        X = (lambda x, axes=(-2, -1): torch.fft.rfft2(x.float(), dim=axes))(x, axes=(-2, -1))
        W = w_r + 1j * w_i
        X = X * W
        y = (lambda x, s, axes=(-2, -1): torch.fft.irfft2(x, s=s, dim=axes).to(torch.bfloat16))(X, (S, D), axes=(-2, -1))
        return y

    def _B_ref(x, mem0, Wq, Wk, Wv, Wo, seg_len):
        B, S, Dm = x.shape
        H, Dh = (Wq.shape[1], Wq.shape[2])
        M = mem0.shape[1]
        scale = Dh ** (-0.5)
        n_seg = S // seg_len
        L = M + seg_len
        idx = torch.arange(L)
        mask = (idx[:, None] < M) | (idx[:, None] >= idx[None, :])
        mem = mem0
        outs = []
        for s in range(n_seg):
            xs = x[:, s * seg_len:(s + 1) * seg_len, :].clone()
            h = torch.concatenate([mem, xs], axis=1)
            q = torch.einsum('btd,dhe->bhte', h, Wq)
            k = torch.einsum('btd,dhe->bhte', h, Wk)
            v = torch.einsum('btd,dhe->bhte', h, Wv)
            scores = torch.einsum('bhqe,bhke->bhqk', q, k) * scale
            scores = torch.where(mask[None, None], scores, -1000000000.0)
            p = _softmax(scores, axis=-1)
            o = torch.einsum('bhqk,bhke->bhqe', p, v)
            o = torch.einsum('bhte,hed->btd', o, Wo)
            mem = o[:, :M, :].clone()
            outs.append(o[:, M:, :])
        return torch.concatenate(outs, axis=1)
    a = _A_ref(A_x, A_w_r, A_w_i)
    b = _B_ref(B_x, B_mem0, B_Wq, B_Wk, B_Wv, B_Wo, B_seg_len)
    return torch.concatenate([a, b], axis=-1)
```

```cpp
#include <hip/hip_runtime.h>
#include <hip/hip_bf16.h>
#include <stdint.h>

typedef __bf16 bf16x2 __attribute__((ext_vector_type(2)));
typedef __bf16 bf16x4 __attribute__((ext_vector_type(4)));
typedef __bf16 bf16x8 __attribute__((ext_vector_type(8)));
typedef __bf16 bf16x16 __attribute__((ext_vector_type(16)));
typedef float f32x2 __attribute__((ext_vector_type(2)));
typedef float f32x4 __attribute__((ext_vector_type(4)));
typedef unsigned u32x2 __attribute__((ext_vector_type(2)));
typedef unsigned u32x3 __attribute__((ext_vector_type(3)));
typedef unsigned u32x4 __attribute__((ext_vector_type(4)));
typedef unsigned u32x8 __attribute__((ext_vector_type(8)));
#define LAS __attribute__((address_space(3)))
#define DI __device__ __forceinline__
#define BC(T, x) __builtin_bit_cast(T, x)
typedef __amdgpu_buffer_rsrc_t rsrc_t;

constexpr unsigned SENT = 0xFFFFFFFFu;
constexpr int SC1 = 16;
constexpr int SPIN_CAP = 1 << 20;

constexpr unsigned CT_KV = 0;
constexpr unsigned CT_M = 8192;
constexpr unsigned CT_O = 24576;
constexpr unsigned CT_F1 = 28672;
constexpr unsigned CT_F2 = 28928;
constexpr unsigned CT_R = 29184;
constexpr unsigned CT_BYTES = 32768;
constexpr unsigned WS_RA = 0;
constexpr unsigned WS_RB = 2u << 20;
constexpr unsigned WS_MRING = WS_RA + 64 * 16384;
constexpr unsigned WS_KX = 4u << 20;
constexpr unsigned WS_VX = 20u << 20;
constexpr unsigned WS_QX = 36u << 20;
constexpr unsigned WS_OX = 52u << 20;
constexpr unsigned WS_KM = 68u << 20;
constexpr unsigned WS_VM = 76u << 20;
constexpr unsigned WS_F = 80u << 20;
constexpr unsigned WS_DBG = 88u << 20;

struct Params {
    const void* Ax; const void* wr; const void* wi; const void* Bx; const void* mem0;
    const void* Wq; const void* Wk; const void* Wv; const void* Wo;
    void* out; char* ctl; char* ws;
    int d1, d2, par, gap;
};

DI unsigned pack2(float a, float b) { f32x2 v = {a, b}; return BC(unsigned, __builtin_convertvector(v, bf16x2)); }
DI void bar() { asm volatile("s_waitcnt lgkmcnt(0)\n\ts_barrier" ::: "memory"); }
DI void drain() { asm volatile("s_waitcnt vmcnt(0)" ::: "memory"); }
DI void dma16(const void* gsrc, unsigned lds_dst) {
    unsigned keep;
    asm volatile("s_mov_b32 %0, m0\n\ts_mov_b32 m0, %2\n\ts_nop 0\n\tglobal_load_lds_dwordx4 %1, off\n\ts_mov_b32 m0, %0" : "=&s"(keep) : "v"(gsrc), "s"(lds_dst) : "memory");
}
DI u32x2 trd(const LAS char* p) { return BC(u32x2, __builtin_amdgcn_ds_read_tr16_b64_v4bf16((LAS bf16x4*)p)); }
DI u32x4 lds128(const LAS char* p) { return *(const LAS u32x4*)p; }
DI f32x4 smf(u32x4 a, const u32x8& b, f32x4 c, int idx) { return __builtin_amdgcn_smfmac_f32_16x16x64_bf16(BC(bf16x8, a), BC(bf16x16, b), c, idx, 0, 0); }
DI f32x4 mf32(u32x4 a, u32x4 b, f32x4 c) { return __builtin_amdgcn_mfma_f32_16x16x32_bf16(BC(bf16x8, a), BC(bf16x8, b), c, 0, 0, 0); }
DI void sleepn(int n) { for (int i = 0; i < n; ++i) __builtin_amdgcn_s_sleep(1); }
DI unsigned opq(unsigned x) { asm volatile("" : "+v"(x)); return x; }
DI float opqf(float x) { asm volatile("" : "+v"(x)); return x; }
DI f32x4 zero4() { f32x4 z = {0, 0, 0, 0}; asm volatile("" : "+v"(z)); return z; }
#define SB() __builtin_amdgcn_sched_barrier(0)
#define PIN1(x) asm volatile("" : "+v"(x))
#define PIN2(x, y) asm volatile("" : "+v"(x), "+v"(y))
#define PINA1(x) asm volatile("" : "+a"(x))
#define PINA2(x, y) asm volatile("" : "+a"(x), "+a"(y))
DI float vmax(float a, float b) { float r; asm("v_max_f32 %0, %1, %2" : "=v"(r) : "v"(a), "v"(b)); return r; }
DI float vmax3(float a, float b, float c) { float r; asm("v_max3_f32 %0, %1, %2, %3" : "=v"(r) : "v"(a), "v"(b), "v"(c)); return r; }
DI float u2f(unsigned x) { return __builtin_bit_cast(float, x); }
DI unsigned f2u(float x) { return __builtin_bit_cast(unsigned, x); }
DI float xmax16(float x) { unsigned a = f2u(x); auto r = __builtin_amdgcn_permlane16_swap(a, opq(a), false, false); return vmax(u2f(r[0]), u2f(r[1])); }
DI float xmax32(float x) { unsigned a = f2u(x); auto r = __builtin_amdgcn_permlane32_swap(a, opq(a), false, false); return vmax(u2f(r[0]), u2f(r[1])); }
DI float xadd16(float x) { unsigned a = f2u(x); auto r = __builtin_amdgcn_permlane16_swap(a, opq(a), false, false); return u2f(r[0]) + u2f(r[1]); }
DI float xadd32(float x) { unsigned a = f2u(x); auto r = __builtin_amdgcn_permlane32_swap(a, opq(a), false, false); return u2f(r[0]) + u2f(r[1]); }
DI unsigned umax3(unsigned a, unsigned b, unsigned c) { return __builtin_elementwise_max(__builtin_elementwise_max(a, b), c); }
DI unsigned smax(unsigned acc, const u32x4& v) { return umax3(umax3(acc, v.x, v.y), v.z, v.w); }
DI unsigned fold(const f32x4& c) { return pack2(opqf(c[0] + c[1]), c[2] + c[3]); }
DI u32x4 ld128(rsrc_t rs, unsigned off) { return __builtin_amdgcn_raw_buffer_load_b128(rs, off, 0, SC1); }
DI u32x2 ld64(rsrc_t rs, unsigned off) { return __builtin_amdgcn_raw_buffer_load_b64(rs, off, 0, SC1); }
DI unsigned ld32(rsrc_t rs, unsigned off) { return __builtin_amdgcn_raw_buffer_load_b32(rs, off, 0, SC1); }
DI void st128(u32x4 v, rsrc_t rs, unsigned off) { __builtin_amdgcn_raw_buffer_store_b128(v, rs, off, 0, SC1); }
DI void st64(u32x2 v, rsrc_t rs, unsigned off) { __builtin_amdgcn_raw_buffer_store_b64(v, rs, off, 0, SC1); }
DI void st32(unsigned v, rsrc_t rs, unsigned off) { __builtin_amdgcn_raw_buffer_store_b32(v, rs, off, 0, SC1); }
DI void wait_flags(rsrc_t rc, unsigned off, int n, int l, int nap) {
    const unsigned o = off + (l < n ? l : 0) * 4;
    for (int spins = 0; spins < SPIN_CAP; ++spins) {
        const unsigned v = ld32(rc, o);
        if (!__any(v == 0)) break;
        for (int i = 0; i < nap; ++i) __builtin_amdgcn_s_sleep(1);
        asm volatile("" ::: "memory");
    }
    asm volatile("" ::: "memory");
}
template <int C> struct IC { static constexpr int v = C; };
template <int I, int N, typename F> DI void static_for(F&& f) { if constexpr (I < N) { f(IC<I>{}); static_for<I + 1, N>(f); } }
constexpr float CS = 0.08838834764831845f * 1.4426950408889634f;

constexpr int L_STG = 0;
constexpr int L_MI = 131072;
constexpr int L_KX = L_MI + 8192;
constexpr int L_ZQ = L_KX + 8192;
constexpr int L_ZK = L_ZQ + 2048;
constexpr int L_O = L_ZK + 4096;
constexpr int L_END = L_O + 1024;
constexpr int LDS_BYTES = L_END;

DI void chain_role(const Params& P, const int h, const int hv, LAS char* lds) {
    const int tid = threadIdx.x, l = tid & 63, w = __builtin_amdgcn_readfirstlane(tid >> 6);
    const int n = l & 15, g = l >> 4;
    const int rank = 2 * h + hv;
    const rsrc_t rs = __builtin_amdgcn_make_buffer_rsrc(P.ws, 0, 0x7fffffff, 0x00020000);
    const rsrc_t rc = __builtin_amdgcn_make_buffer_rsrc(P.ctl, 0, CT_BYTES, 0x00020000);
    const unsigned ldsbase = (unsigned)(size_t)lds;
    const u32x4 s4 = {SENT, SENT, SENT, SENT};
    const unsigned myin0 = WS_RA + P.par + rank * 16384, myin1 = WS_RB + P.par + rank * 16384;
    const unsigned mring = WS_MRING + P.par;
#pragma unroll
    for (int i = 0; i < 2; ++i) { st128(s4, rs, myin0 + tid * 16 + i * 4096); st128(s4, rs, myin1 + tid * 16 + i * 4096); }
    if (tid < 32) st128(s4, rs, mring + (tid >> 3) * 16384 + rank * 128 + (tid & 7) * 16);

    u32x8 Wq[2][8], Wk[2][8], Wv[8], Wo[8];
    {
        const int l2 = (l >> 2) & 1, l3 = (l >> 3) & 1, l4 = (l >> 4) & 1, l5 = (l >> 5) & 1;
        const int unit = ((((l >> 1) & 1) ^ l4) * 32) + (l & 1) * 16;
        const int dl = l2 + 16 * l3 + 32 * l4 + 2 * l5;
        const int el = l2 + 8 * l3 + 16 * l4 + 2 * l5;
        const char* sq = (const char*)P.Wq + (size_t)dl * 8192 + h * 256 + w * 64 + unit;
        const char* sk = (const char*)P.Wk + (size_t)dl * 8192 + h * 256 + w * 64 + unit;
        const char* sv = (const char*)P.Wv + (size_t)dl * 8192 + h * 256 + (2 * hv + (w >> 1)) * 64 + unit;
        const char* so = (const char*)P.Wo + (size_t)(h * 128 + 64 * hv + el) * 1024 + w * 256 + unit;
        const unsigned stg = __builtin_amdgcn_readfirstlane(ldsbase + L_STG + w * 32768);
        auto issue = [&](auto cc) {
            constexpr int c = decltype(cc)::v;
            const unsigned dst = stg + (c & 7) * 4096;
#pragma unroll
            for (int i = 0; i < 4; ++i) {
                const char* src;
                if (c < 24) { const char* b = c < 8 ? sq : c < 16 ? sk : sv; src = b + (size_t)(64 * (c & 7) + 4 * (i & 1) + 8 * (i >> 1)) * 8192; }
                else src = so + (c - 24) * 64 + (size_t)(4 * (i & 1) + 32 * (i >> 1)) * 1024;
                dma16(src, dst + i * 1024);
            }
        };
        const int qq = (l >> 2) & 3, p = l & 3;
        const int lam = (qq & 1) + 2 * (g & 1) + 4 * (qq >> 1) + 16 * (g >> 1), lam2 = qq >> 1;
        const LAS char* rd0 = lds + L_STG + w * 32768 + lam * 64 + ((0 ^ lam2) * 32) + 8 * p;
        const LAS char* rd1 = lds + L_STG + w * 32768 + lam * 64 + ((1 ^ lam2) * 32) + 8 * p;
        const LAS char* rdv = lds + L_STG + w * 32768 + lam * 64 + (((p >> 1) ^ lam2) * 32) + (p & 1) * 16 + 8 * (w & 1);
        auto frag = [&](const LAS char* b, int slot) {
            const LAS char* a = b + slot * 4096;
            const u32x2 r0 = trd(a), r1 = trd(a + 512), r2 = trd(a + 2048), r3 = trd(a + 2560);
            const u32x8 f = {r0.x, r0.y, r1.x, r1.y, r2.x, r2.y, r3.x, r3.y};
            return f;
        };
        static_for<0, 8>(issue);
        static_for<0, 28>([&](auto cc) {
            constexpr int c = decltype(cc)::v;
            constexpr int newer = (c + 7 <= 27 ? 7 : 27 - c);
            asm volatile("s_waitcnt vmcnt(%0)" :: "n"(4 * newer) : "memory");
            if constexpr (c == 0) {
                bar();
                if (tid == 0) st32(1u, rc, CT_R + rank * 4);
            }
            if constexpr (c < 8) { Wq[0][c] = frag(rd0, c & 7); Wq[1][c] = frag(rd1, c & 7); }
            else if constexpr (c < 16) { Wk[0][c - 8] = frag(rd0, c & 7); Wk[1][c - 8] = frag(rd1, c & 7); }
            else if constexpr (c < 24) { Wv[c - 16] = frag(rdv, c & 7); }
            else { Wo[2 * (c - 24)] = frag(rd0, c & 7); Wo[2 * (c - 24) + 1] = frag(rd1, c & 7); }
            asm volatile("s_waitcnt lgkmcnt(0)" ::: "memory");
            if constexpr (c + 8 <= 27) issue(IC<c + 8>{});
        });
    }

#pragma unroll
    for (int k = 0; k < 2; ++k) {
        const int ch = tid + 256 * k, cg = ch >> 4, rho = ch & 15, i = rho >> 1, hf = rho & 1;
        const char* src = (const char*)P.mem0 + i * 1024 + (64 * (cg >> 2) + 32 * hf + 4 * (cg & 3)) * 2;
        const u32x2 a = *(const u32x2*)src, b = *(const u32x2*)(src + 32);
        *(LAS u32x4*)(lds + L_MI + ch * 16) = (u32x4){a.x, a.y, b.x, b.y};
    }
    *(LAS u32x4*)(lds + L_ZK + tid * 16) = (u32x4){0, 0, 0, 0};
    wait_flags(rc, CT_R, 64, l, 1);
    unsigned long long kvmask = 0;
    auto need_kv = [&](int s) {
        for (int spins = 0; !((kvmask >> s) & 1) && spins < SPIN_CAP; ++spins) {
            kvmask = __ballot(ld32(rc, CT_KV + h * 256 + l * 4) != 0);
            asm volatile("" ::: "memory");
        }
    };
    need_kv(0);
    const unsigned kxo = WS_KX + h * 8192 + w * 2048 + l * 16;
    const unsigned vxo = WS_VX + h * 8192 + (4 * hv + w) * 1024 + l * 16;
    u32x4 vx;
    {
        const u32x4 k0 = ld128(rs, kxo), k1 = ld128(rs, kxo + 1024);
        vx = ld128(rs, vxo);
        *(LAS u32x4*)(lds + L_KX + w * 2048 + l * 16) = k0; *(LAS u32x4*)(lds + L_KX + w * 2048 + 1024 + l * 16) = k1;
    }
    const int idx = (n & 1) ? 0xEEEE : 0x4444;
    const int qq = (l >> 2) & 3, p4 = l & 3;
    const LAS char* aMI = lds + L_MI + l * 16;
    const LAS char* aKX = lds + L_KX + l * 16;
    LAS char* wZQ = lds + L_ZQ + (32 * w + n) * 16 + 4 * g;
    LAS char* wZK = lds + L_ZK + (32 * w + n) * 32 + 8 * g;
    const LAS char* rZQ = lds + L_ZQ + (8 * g + qq) * 16 + (p4 & 1) * 8;
    const LAS char* rZK = lds + L_ZK + (8 * g + qq) * 32 + p4 * 8;
    const LAS char* rZKp = rZK + (2 * hv + (w >> 1)) * 1024 + (w & 1) * 128;
    LAS char* wO = lds + L_O + (n & 7) * 128 + (16 * w + 4 * g) * 2;
    const LAS char* rO = lds + L_O + (n >> 1) * 128 + g * 32 + (n & 1) * 16;
    const unsigned inb0 = WS_RA + P.par + (2 * (8 * w + (n >> 2)) + (g >> 1)) * 16384 + rank * 128 + ((g & 1) * 4 + (n & 3)) * 16;
    const unsigned kmo = WS_KM + h * 4096 + (2 * hv + (w >> 1)) * 1024 + (w & 1) * 512 + l * 8;
    const unsigned vmo = WS_VM + h * 2048 + (4 * hv + w) * 256 + l * 4;
    const u32x4 sel = {n == 0 ? 0x3F80u : n == 1 ? 0x3F800000u : 0u, n == 2 ? 0x3F80u : n == 3 ? 0x3F800000u : 0u,
                       n == 4 ? 0x3F80u : n == 5 ? 0x3F800000u : 0u, n == 6 ? 0x3F80u : n == 7 ? 0x3F800000u : 0u};
    const unsigned pubo = (rank >> 1) * 256 + (2 * (4 * (rank & 1) + 2 * g + (n & 1)) + (n >> 2)) * 16 + ((n >> 1) & 1) * 8;
    bar();
    for (int s = 0;; ++s) {
        u32x4 a[8];
#pragma unroll
        for (int ks = 0; ks < 8; ++ks) a[ks] = lds128(aMI + ks * 1024);
        f32x4 q0 = zero4(), q1 = zero4(), k0 = zero4(), k1 = zero4();
        SB();
#pragma unroll
        for (int ks = 0; ks < 8; ++ks) { q0 = smf(a[ks], Wq[0][ks], q0, idx); q1 = smf(a[ks], Wq[1][ks], q1, idx); }
        PIN2(q0, q1); SB();
        k0 = smf(a[0], Wk[0][0], k0, idx); k1 = smf(a[0], Wk[1][0], k1, idx);
        PIN2(k0, k1); SB();
        { const unsigned z0 = fold(q0); *(LAS unsigned*)(wZQ) = z0; }
        k0 = smf(a[1], Wk[0][1], k0, idx);
        PIN1(k0); SB();
        { const unsigned z1 = fold(q1); *(LAS unsigned*)(wZQ + 256) = z1; }
        k1 = smf(a[1], Wk[1][1], k1, idx);
        PIN1(k1); SB();
        k0 = smf(a[2], Wk[0][2], k0, idx); k1 = smf(a[2], Wk[1][2], k1, idx);
        PIN2(k0, k1); SB();
        bar();
        u32x4 qb[4];
#pragma unroll
        for (int kk = 0; kk < 4; ++kk) { const u32x2 x = trd(rZQ + kk * 512), y = trd(rZQ + kk * 512 + 64); qb[kk] = (u32x4){x.x, x.y, y.x, y.y}; }
        SB();
        k0 = smf(a[3], Wk[0][3], k0, idx); k1 = smf(a[3], Wk[1][3], k1, idx);
        PIN2(k0, k1); SB();
        u32x4 kx0[4], kx1[4];
        kx0[0] = lds128(aKX); kx1[0] = lds128(aKX + 4096);
        kx0[1] = lds128(aKX + 1024); kx1[1] = lds128(aKX + 4096 + 1024);
        SB();
        k0 = smf(a[4], Wk[0][4], k0, idx); k1 = smf(a[4], Wk[1][4], k1, idx);
        k0 = smf(a[5], Wk[0][5], k0, idx); k1 = smf(a[5], Wk[1][5], k1, idx);
        PIN2(k0, k1); SB();
        kx0[2] = lds128(aKX + 2048); kx1[2] = lds128(aKX + 4096 + 2048);
        kx0[3] = lds128(aKX + 3072); kx1[3] = lds128(aKX + 4096 + 3072);
        SB();
        k0 = smf(a[6], Wk[0][6], k0, idx); k1 = smf(a[6], Wk[1][6], k1, idx);
        k0 = smf(a[7], Wk[0][7], k0, idx); k1 = smf(a[7], Wk[1][7], k1, idx);
        PIN2(k0, k1); SB();
        f32x4 S0 = {0, 0, 0, 0}, S1 = S0, S2 = S0;
        S0 = mf32(kx0[0], qb[0], S0); S1 = mf32(kx1[0], qb[0], S1);
        PINA2(S0, S1); SB();
        { const unsigned z0 = fold(k0); *(LAS unsigned*)(wZK) = z0; }
        S0 = mf32(kx0[1], qb[1], S0);
        PINA1(S0); SB();
        { const unsigned z1 = fold(k1); *(LAS unsigned*)(wZK + 512) = z1; }
        S1 = mf32(kx1[1], qb[1], S1);
        PINA1(S1); SB();
        S0 = mf32(kx0[2], qb[2], S0); S1 = mf32(kx1[2], qb[2], S1);
        PINA2(S0, S1); SB();
        bar();
        u32x4 km[4];
#pragma unroll
        for (int kk = 0; kk < 2; ++kk) { const u32x2 x = trd(rZK + kk * 1024), y = trd(rZK + kk * 1024 + 128); km[kk] = (u32x4){x.x, x.y, y.x, y.y}; }
        SB();
        S0 = mf32(kx0[3], qb[3], S0);
        PINA1(S0); SB();
#pragma unroll
        for (int kk = 2; kk < 4; ++kk) { const u32x2 x = trd(rZK + kk * 1024), y = trd(rZK + kk * 1024 + 128); km[kk] = (u32x4){x.x, x.y, y.x, y.y}; }
        SB();
        S1 = mf32(kx1[3], qb[3], S1);
        PINA1(S1); SB();
        a[0] = lds128(aMI); a[1] = lds128(aMI + 1024); a[2] = lds128(aMI + 2048); a[3] = lds128(aMI + 3072);
        f32x4 v0 = zero4();
        SB();
        float m = vmax(vmax3(vmax3(S0[0], S0[1], S0[2]), S0[3], S1[0]), vmax3(S1[1], S1[2], S1[3]));
        PIN1(m); SB();
        S2 = mf32(km[0], qb[0], S2); S2 = mf32(km[1], qb[1], S2);
        PINA1(S2); SB();
        a[4] = lds128(aMI + 4096); a[5] = lds128(aMI + 5120);
        SB();
        S2 = mf32(km[2], qb[2], S2); S2 = mf32(km[3], qb[3], S2);
        PINA1(S2); SB();
        a[6] = lds128(aMI + 6144); a[7] = lds128(aMI + 7168);
        const u32x2 kmp = trd(rZKp);
        SB();
        v0 = smf(a[0], Wv[0], v0, idx);
        PIN1(v0); SB();
        v0 = smf(a[1], Wv[1], v0, idx);
        PIN1(v0); SB();
        m = vmax3(m, S2[0], S2[1]);
        m = xmax16(m);
        PIN1(m); SB();
        v0 = smf(a[2], Wv[2], v0, idx);
        PIN1(v0); SB();
        m = xmax32(m);
        float mc = m * CS;
        PIN1(mc); SB();
        float p0[4], p1[4], p2[2];
        v0 = smf(a[3], Wv[3], v0, idx);
        PIN1(v0); SB();
        p0[0] = __builtin_amdgcn_exp2f(S0[0] * CS - mc); p0[1] = __builtin_amdgcn_exp2f(S0[1] * CS - mc);
        PIN2(p0[0], p0[1]); SB();
        v0 = smf(a[4], Wv[4], v0, idx);
        PIN1(v0); SB();
        p0[2] = __builtin_amdgcn_exp2f(S0[2] * CS - mc); p0[3] = __builtin_amdgcn_exp2f(S0[3] * CS - mc);
        PIN2(p0[2], p0[3]); SB();
        v0 = smf(a[5], Wv[5], v0, idx);
        PIN1(v0); SB();
        p1[0] = __builtin_amdgcn_exp2f(S1[0] * CS - mc); p1[1] = __builtin_amdgcn_exp2f(S1[1] * CS - mc);
        PIN2(p1[0], p1[1]); SB();
        v0 = smf(a[6], Wv[6], v0, idx);
        PIN1(v0); SB();
        p1[2] = __builtin_amdgcn_exp2f(S1[2] * CS - mc); p1[3] = __builtin_amdgcn_exp2f(S1[3] * CS - mc);
        PIN2(p1[2], p1[3]); SB();
        v0 = smf(a[7], Wv[7], v0, idx);
        PIN1(v0); SB();
        p2[0] = __builtin_amdgcn_exp2f(S2[0] * CS - mc); p2[1] = __builtin_amdgcn_exp2f(S2[1] * CS - mc);
        u32x4 pb = {pack2(p0[0], p0[1]), pack2(p0[2], p0[3]), pack2(p1[0], p1[1]), pack2(p1[2], p1[3])};
        PIN1(pb); SB();
        f32x4 ot = {0, 0, 0, 0};
        ot = mf32(vx, pb, ot);
        PINA1(ot); SB();
        const unsigned vmd = fold(v0);
        const u32x4 pb2 = {pack2(p2[0], p2[1]), 0u, 0u, 0u};
        float ls = ((p0[0] + p0[1]) + (p0[2] + p0[3])) + ((p1[0] + p1[1]) + (p1[2] + p1[3]));
        ot = mf32((u32x4){vmd, 0u, 0u, 0u}, pb2, ot);
        PINA1(ot); SB();
        ls += p2[0] + p2[1];
        ls = xadd32(xadd16(ls));
        const float il = __builtin_amdgcn_rcpf(ls);
        SB();
        *(LAS u32x2*)(wO) = (u32x2){pack2(opqf(ot[0] * il), ot[1] * il), pack2(opqf(ot[2] * il), ot[3] * il)};
        SB();
        if (s == 63) {
            st64(kmp, rs, kmo + s * (32 * 4096));
            st32(vmd, rs, vmo + s * (32 * 2048));
            break;
        }
        bar();
        const u32x4 oa = lds128(rO);
        SB();
        f32x4 c[8];
#pragma unroll
        for (int t = 0; t < 8; ++t) c[t] = zero4();
        unsigned d[8];
        SB();
        c[0] = smf(oa, Wo[0], c[0], idx); c[1] = smf(oa, Wo[1], c[1], idx); c[2] = smf(oa, Wo[2], c[2], idx);
        PIN2(c[0], c[1]); PIN1(c[2]); SB();
#pragma unroll
        for (int t = 0; t < 5; ++t) { c[t + 3] = smf(oa, Wo[t + 3], c[t + 3], idx); PIN1(c[t + 3]); SB(); d[t] = fold(c[t]); PIN1(d[t]); SB(); }
        const unsigned slot = (s & 1) * (WS_RB - WS_RA);
        st128((u32x4){d[0], d[1], d[2], d[3]}, rs, inb0 + slot);
        d[5] = fold(c[5]); d[6] = fold(c[6]); d[7] = fold(c[7]);
        st128((u32x4){d[4], d[5], d[6], d[7]}, rs, inb0 + slot + 8 * 16384);
        SB();
        st64(kmp, rs, kmo + s * (32 * 4096));
        st32(vmd, rs, vmo + s * (32 * 2048));
        if (s > 0 && tid == 64) st32(1u, rc, CT_M + ((s - 1) * 64 + rank) * 4);
        need_kv(s + 1);
        vx = ld128(rs, vxo + (s + 1) * (32 * 8192));
        const u32x4 kn0 = ld128(rs, kxo + (s + 1) * (32 * 8192));
        const u32x4 kn1 = ld128(rs, kxo + (s + 1) * (32 * 8192) + 1024);
        const unsigned mnext = mring + ((s + 1) & 3) * 16384;
        SB();
        if (w == 0) {
            sleepn(P.d2);
            const unsigned gb = myin0 + slot + l * 16;
            f32x4 R; int spins = 0;
            for (;;) {
                u32x4 x[8];
#pragma unroll
                for (int k = 0; k < 8; ++k) x[k] = __builtin_amdgcn_raw_buffer_load_b128(rs, gb + (k & 3) * 1024, (k >> 2) * 4096, SC1);
                R = (f32x4){0, 0, 0, 0};
                unsigned mx = 0;
#pragma unroll
                for (int k = 0; k < 8; ++k) { R = mf32(x[k], sel, R); mx = smax(mx, x[k]); }
                if (!__any(mx == SENT)) break;
                if (++spins > SPIN_CAP) break;
                asm volatile("" ::: "memory");
            }
            auto a02 = __builtin_amdgcn_permlane32_swap(f2u(R[0]), f2u(R[2]), false, false);
            auto a13 = __builtin_amdgcn_permlane32_swap(f2u(R[1]), f2u(R[3]), false, false);
            const unsigned dd = pack2(opqf(u2f(a02[0]) + u2f(a02[1])), u2f(a13[0]) + u2f(a13[1]));
            auto de = __builtin_amdgcn_permlane32_swap(dd, opq(dd), false, false);
            if (l < 32 && n < 8) st64((u32x2){de[0], de[1]}, rs, mnext + pubo);
        }
        bar();
        {
            const unsigned b = myin0 + slot + w * 2048 + l * 16;
            st128(s4, rs, b); st128(s4, rs, b + 1024);
            if (w == 0 && s > 0 && l < 8) st128(s4, rs, mring + (s & 3) * 16384 + rank * 128 + l * 16);
        }
        *(LAS u32x4*)(lds + L_KX + w * 2048 + l * 16) = kn0; *(LAS u32x4*)(lds + L_KX + w * 2048 + 1024 + l * 16) = kn1;
        sleepn(P.d1);
        {
            u32x4 m0, m1; int spins = 0;
            for (;;) {
                m0 = ld128(rs, mnext + w * 2048 + l * 16);
                m1 = ld128(rs, mnext + w * 2048 + 1024 + l * 16);
                if (!__any(smax(smax(0u, m0), m1) == SENT)) break;
                if (++spins > SPIN_CAP) break;
                asm volatile("" ::: "memory");
            }
            *(LAS u32x4*)(lds + L_MI + w * 2048 + l * 16) = m0; *(LAS u32x4*)(lds + L_MI + w * 2048 + 1024 + l * 16) = m1;
        }
        bar();
    }
    drain();
    bar();
    if (tid == 0) { st32(1u, rc, CT_M + (62 * 64 + rank) * 4); st32(1u, rc, CT_M + (63 * 64 + rank) * 4); }
}

DI void lead_role(const Params& P, const int h, const int c, LAS char* lds) {
    const int tid = threadIdx.x, l = tid & 63, w = __builtin_amdgcn_readfirstlane(tid >> 6);
    const int g = l >> 4;
    const rsrc_t rs = __builtin_amdgcn_make_buffer_rsrc(P.ws, 0, 0x7fffffff, 0x00020000);
    const rsrc_t rc = __builtin_amdgcn_make_buffer_rsrc(P.ctl, 0, CT_BYTES, 0x00020000);
    const unsigned ldsbase = (unsigned)(size_t)lds;
    u32x4 WK[2][16], WV[2][16], WQ[2][16];
    {
        const size_t lo = (size_t)(l >> 2) * 8192 + h * 256 + w * 64 + (l & 3) * 16;
        const char* sk = (const char*)P.Wk + lo; const char* sv = (const char*)P.Wv + lo; const char* sq = (const char*)P.Wq + lo;
        const unsigned stg = __builtin_amdgcn_readfirstlane(ldsbase + L_STG + w * 32768);
        auto issue = [&](auto cc) {
            constexpr int ch = decltype(cc)::v;
            const char* b = ch < 8 ? sk : ch < 16 ? sv : sq;
#pragma unroll
            for (int i = 0; i < 4; ++i) dma16(b + (size_t)(64 * (ch & 7) + 16 * i) * 8192, stg + (ch & 7) * 4096 + i * 1024);
        };
        const LAS char* rd = lds + L_STG + w * 32768 + (8 * g + ((l >> 2) & 3)) * 64 + (l & 3) * 16;
        auto frag = [&](int slot, int kdl, int tb) {
            const LAS char* a = rd + slot * 4096 + kdl * 2048 + tb * 8;
            const u32x2 r0 = trd(a), r1 = trd(a + 256);
            return (u32x4){r0.x, r0.y, r1.x, r1.y};
        };
        static_for<0, 8>(issue);
        static_for<0, 24>([&](auto cc) {
            constexpr int ch = decltype(cc)::v;
            constexpr int newer = (ch + 7 <= 23 ? 7 : 23 - ch);
            asm volatile("s_waitcnt vmcnt(%0)" :: "n"(4 * newer) : "memory");
#pragma unroll
            for (int kdl = 0; kdl < 2; ++kdl)
#pragma unroll
                for (int tb = 0; tb < 2; ++tb) {
                    const u32x4 f = frag(ch & 7, kdl, tb);
                    if constexpr (ch < 8) WK[tb][2 * ch + kdl] = f;
                    else if constexpr (ch < 16) WV[tb][2 * (ch - 8) + kdl] = f;
                    else WQ[tb][2 * (ch - 16) + kdl] = f;
                }
            asm volatile("s_waitcnt lgkmcnt(0)" ::: "memory");
            if constexpr (ch + 8 <= 23) issue(IC<ch + 8>{});
        });
    }
    bar();
    const char* xs = (const char*)P.Bx + (size_t)(16 * (w >> 1) + (l & 15)) * 1024 + ((w & 1) * 8 * 32 + 8 * g) * 2;
    auto issue_x = [&](int s, int buf) {
        const unsigned dst = __builtin_amdgcn_readfirstlane(ldsbase + buf * 32768 + w * 8192);
#pragma unroll
        for (int i = 0; i < 8; ++i) dma16(xs + (size_t)s * 32768 + i * 64, dst + i * 1024);
    };
    issue_x(c, 0);
    for (int u = 0; u < 32; ++u) {
        const int s = 2 * u + c;
        drain();
        bar();
        if (u > 0 && tid == 0) st32(1u, rc, CT_KV + (h * 64 + s - 2) * 4);
        if (u < 31) issue_x(s + 2, (u + 1) & 1);
        const LAS char* xb = lds + (u & 1) * 32768 + l * 16;
        f32x4 K[2][2], Q[2][2], V[2][2];
#pragma unroll
        for (int i = 0; i < 2; ++i)
#pragma unroll
            for (int j = 0; j < 2; ++j) { K[i][j] = (f32x4){0, 0, 0, 0}; Q[i][j] = K[i][j]; V[i][j] = K[i][j]; }
#pragma unroll
        for (int kd = 0; kd < 16; ++kd) {
#pragma unroll
            for (int T = 0; T < 2; ++T) {
                const u32x4 xf = lds128(xb + (16 * T + kd) * 1024);
#pragma unroll
                for (int tb = 0; tb < 2; ++tb) {
                    K[tb][T] = mf32(WK[tb][kd], xf, K[tb][T]);
                    V[tb][T] = mf32(xf, WV[tb][kd], V[tb][T]);
                    Q[tb][T] = mf32(WQ[tb][kd], xf, Q[tb][T]);
                }
            }
        }
        const unsigned blk = (s * 32 + h) * 8192 + l * 16;
#pragma unroll
        for (int T = 0; T < 2; ++T) {
            st128((u32x4){pack2(K[0][T][0], K[0][T][1]), pack2(K[0][T][2], K[0][T][3]), pack2(K[1][T][0], K[1][T][1]), pack2(K[1][T][2], K[1][T][3])}, rs, WS_KX + blk + (4 * T + w) * 1024);
            st128((u32x4){pack2(Q[0][T][0], Q[0][T][1]), pack2(Q[0][T][2], Q[0][T][3]), pack2(Q[1][T][0], Q[1][T][1]), pack2(Q[1][T][2], Q[1][T][3])}, rs, WS_QX + blk + (4 * T + w) * 1024);
        }
#pragma unroll
        for (int tb = 0; tb < 2; ++tb)
            st128((u32x4){pack2(V[tb][0][0], V[tb][0][1]), pack2(V[tb][0][2], V[tb][0][3]), pack2(V[tb][1][0], V[tb][1][1]), pack2(V[tb][1][2], V[tb][1][3])}, rs, WS_VX + blk + (2 * w + tb) * 1024);
    }
    drain();
    bar();
    if (tid == 0) st32(1u, rc, CT_KV + (h * 64 + 62 + c) * 4);
}

constexpr int L_RED = 0;
DI void tail_role(const Params& P, const int xg, const int cs, LAS char* lds) {
    const int tid = threadIdx.x, l = tid & 63, w = __builtin_amdgcn_readfirstlane(tid >> 6);
    const int n = l & 15, g = l >> 4;
    const rsrc_t rs = __builtin_amdgcn_make_buffer_rsrc(P.ws, 0, 0x7fffffff, 0x00020000);
    const rsrc_t rc = __builtin_amdgcn_make_buffer_rsrc(P.ctl, 0, CT_BYTES, 0x00020000);
    const unsigned ldsbase = (unsigned)(size_t)lds;
    u32x4 WO[2][32];
    {
        const char* so = (const char*)P.Wo + (size_t)(1024 * w + (l >> 2)) * 1024 + cs * 64 + (((l >> 1) & 1) ^ ((l >> 5) & 1)) * 32 + (l & 1) * 16;
        const unsigned stg = __builtin_amdgcn_readfirstlane(ldsbase + L_STG + w * 32768);
        auto issue = [&](auto cc) {
            constexpr int ch = decltype(cc)::v;
#pragma unroll
            for (int i = 0; i < 4; ++i) dma16(so + (size_t)(64 * ch + 16 * i) * 1024, stg + (ch & 7) * 4096 + i * 1024);
        };
        const LAS char* rd = lds + L_STG + w * 32768 + (8 * g + ((l >> 2) & 3)) * 64 + (l & 3) * 8;
        auto frag = [&](int slot, int kdl, int tn) {
            const LAS char* a = rd + slot * 4096 + kdl * 2048 + ((tn ^ (g & 1)) * 32);
            const u32x2 r0 = trd(a), r1 = trd(a + 256);
            return (u32x4){r0.x, r0.y, r1.x, r1.y};
        };
        static_for<0, 8>(issue);
        static_for<0, 16>([&](auto cc) {
            constexpr int ch = decltype(cc)::v;
            constexpr int newer = (ch + 7 <= 15 ? 7 : 15 - ch);
            asm volatile("s_waitcnt vmcnt(%0)" :: "n"(4 * newer) : "memory");
#pragma unroll
            for (int kdl = 0; kdl < 2; ++kdl)
#pragma unroll
                for (int tn = 0; tn < 2; ++tn) WO[tn][2 * ch + kdl] = frag(ch & 7, kdl, tn);
            asm volatile("s_waitcnt lgkmcnt(0)" ::: "memory");
            if constexpr (ch + 8 <= 15) issue(IC<ch + 8>{});
        });
    }
    bar();
    const int hh = 2 * cs + (w >> 1), T = w & 1;
    const int qi = 16 * T + n;
    for (int s = xg; s < 64; s += 4) {
        wait_flags(rc, CT_KV + (hh * 64 + s) * 4, 1, l, 4);
        const unsigned blk = (s * 32 + hh) * 8192 + l * 16;
        u32x4 qb[4], kx[2][4], vx[8];
#pragma unroll
        for (int kk = 0; kk < 4; ++kk) { qb[kk] = ld128(rs, WS_QX + blk + (4 * T + kk) * 1024); kx[0][kk] = ld128(rs, WS_KX + blk + kk * 1024); kx[1][kk] = ld128(rs, WS_KX + blk + (4 + kk) * 1024); }
#pragma unroll
        for (int t = 0; t < 8; ++t) vx[t] = ld128(rs, WS_VX + blk + t * 1024);
        f32x4 S0 = {0, 0, 0, 0}, S1 = S0, S2 = S0;
#pragma unroll
        for (int kk = 0; kk < 4; ++kk) { S0 = mf32(kx[0][kk], qb[kk], S0); S1 = mf32(kx[1][kk], qb[kk], S1); }
        wait_flags(rc, CT_M + (s * 64 + 2 * hh) * 4, 2, l, 2);
        unsigned vm[8];
#pragma unroll
        for (int kk = 0; kk < 4; ++kk) {
            const unsigned o = WS_KM + (s * 32 + hh) * 4096 + kk * 1024 + l * 8;
            const u32x2 x = ld64(rs, o), y = ld64(rs, o + 512);
            S2 = mf32((u32x4){x.x, x.y, y.x, y.y}, qb[kk], S2);
        }
#pragma unroll
        for (int t = 0; t < 8; ++t) vm[t] = ld32(rs, WS_VM + (s * 32 + hh) * 2048 + t * 256 + l * 4);
        float s0[4], s1[4];
#pragma unroll
        for (int r = 0; r < 4; ++r) { s0[r] = (4 * g + r <= qi) ? S0[r] : -__builtin_inff(); s1[r] = (16 + 4 * g + r <= qi) ? S1[r] : -__builtin_inff(); }
        float m = vmax3(vmax3(vmax3(s0[0], s0[1], s0[2]), s0[3], s1[0]), vmax3(s1[1], s1[2], s1[3]), vmax(S2[0], S2[1]));
        m = xmax32(xmax16(m));
        const float mc = m * CS;
        float p0[4], p1[4], p2[2];
#pragma unroll
        for (int r = 0; r < 4; ++r) { p0[r] = __builtin_amdgcn_exp2f(s0[r] * CS - mc); p1[r] = __builtin_amdgcn_exp2f(s1[r] * CS - mc); }
        p2[0] = __builtin_amdgcn_exp2f(S2[0] * CS - mc); p2[1] = __builtin_amdgcn_exp2f(S2[1] * CS - mc);
        const u32x4 pb = {pack2(p0[0], p0[1]), pack2(p0[2], p0[3]), pack2(p1[0], p1[1]), pack2(p1[2], p1[3])};
        const u32x4 pb2 = {pack2(p2[0], p2[1]), 0u, 0u, 0u};
        float ls = ((p0[0] + p0[1]) + (p0[2] + p0[3])) + ((p1[0] + p1[1]) + (p1[2] + p1[3])) + (p2[0] + p2[1]);
        ls = xadd32(xadd16(ls));
        const float il = __builtin_amdgcn_rcpf(ls);
#pragma unroll
        for (int kk = 0; kk < 4; ++kk) {
            f32x4 oa = {0, 0, 0, 0}, ob = oa;
            oa = mf32(vx[2 * kk], pb, oa); ob = mf32(vx[2 * kk + 1], pb, ob);
            oa = mf32((u32x4){vm[2 * kk], 0u, 0u, 0u}, pb2, oa); ob = mf32((u32x4){vm[2 * kk + 1], 0u, 0u, 0u}, pb2, ob);
            st128((u32x4){pack2(oa[0] * il, oa[1] * il), pack2(oa[2] * il, oa[3] * il), pack2(ob[0] * il, ob[1] * il), pack2(ob[2] * il, ob[3] * il)}, rs,
                  WS_OX + s * 262144 + (T * 128 + 4 * hh + kk) * 1024 + l * 16);
        }
        drain();
        bar();
        if (tid == 0) st32(1u, rc, CT_O + (s * 16 + cs) * 4);
        wait_flags(rc, CT_O + s * 64, 16, l, 1);
        f32x4 C[2][2];
#pragma unroll
        for (int i = 0; i < 2; ++i) { C[i][0] = (f32x4){0, 0, 0, 0}; C[i][1] = C[i][0]; }
        const unsigned ab = WS_OX + s * 262144 + 32 * w * 1024 + l * 16;
#pragma unroll
        for (int k0 = 0; k0 < 32; k0 += 4) {
            u32x4 A[2][4];
#pragma unroll
            for (int j = 0; j < 4; ++j) { A[0][j] = ld128(rs, ab + (k0 + j) * 1024); A[1][j] = ld128(rs, ab + (128 + k0 + j) * 1024); }
#pragma unroll
            for (int j = 0; j < 4; ++j)
#pragma unroll
                for (int Tq = 0; Tq < 2; ++Tq) { C[Tq][0] = mf32(A[Tq][j], WO[0][k0 + j], C[Tq][0]); C[Tq][1] = mf32(A[Tq][j], WO[1][k0 + j], C[Tq][1]); }
        }
        LAS float* red = (LAS float*)(lds + L_RED) + w * (32 * 33);
#pragma unroll
        for (int Tq = 0; Tq < 2; ++Tq)
#pragma unroll
            for (int tn = 0; tn < 2; ++tn)
#pragma unroll
                for (int r = 0; r < 4; ++r) red[(16 * Tq + 4 * g + r) * 33 + 16 * tn + n] = C[Tq][tn][r];
        bar();
        {
            const int tok = tid >> 3, c0 = (tid & 7) * 4;
            const LAS float* rr = (const LAS float*)(lds + L_RED) + tok * 33 + c0;
            float o[4];
#pragma unroll
            for (int j = 0; j < 4; ++j) o[j] = (rr[j] + rr[32 * 33 + j]) + (rr[2 * 32 * 33 + j] + rr[3 * 32 * 33 + j]);
            *(u32x2*)((char*)P.out + ((size_t)(32 * s + tok) * 1024 + 512 + 32 * cs + c0) * 2) = (u32x2){pack2(o[0], o[1]), pack2(o[2], o[3])};
        }
        bar();
    }
}

struct cf { float x, y; };
DI cf cadd(cf a, cf b) { return {a.x + b.x, a.y + b.y}; }
DI cf csub(cf a, cf b) { return {a.x - b.x, a.y - b.y}; }
DI cf cmul(cf a, cf b) { return {a.x * b.x - a.y * b.y, a.x * b.y + a.y * b.x}; }
template <bool INV> DI cf crot(cf a) { return INV ? cf{-a.y, a.x} : cf{a.y, -a.x}; }
template <bool INV> DI void fft4(cf& a0, cf& a1, cf& a2, cf& a3) {
    const cf s02 = cadd(a0, a2), d02 = csub(a0, a2), s13 = cadd(a1, a3), d13 = crot<INV>(csub(a1, a3));
    a0 = cadd(s02, s13); a1 = cadd(d02, d13); a2 = csub(s02, s13); a3 = csub(d02, d13);
}
template <bool INV> DI void fft8(cf (&v)[8]) {
    fft4<INV>(v[0], v[2], v[4], v[6]);
    fft4<INV>(v[1], v[3], v[5], v[7]);
    constexpr float h = 0.70710678118654752f;
    const cf o0 = v[1];
    const cf r1 = crot<INV>(v[3]); const cf o1 = {h * (v[3].x + r1.x), h * (v[3].y + r1.y)};
    const cf o2 = crot<INV>(v[5]);
    const cf r3 = crot<INV>(v[7]); const cf o3 = {h * (r3.x - v[7].x), h * (r3.y - v[7].y)};
    const cf e0 = v[0], e1 = v[2], e2 = v[4], e3 = v[6];
    v[0] = cadd(e0, o0); v[1] = cadd(e1, o1); v[2] = cadd(e2, o2); v[3] = cadd(e3, o3);
    v[4] = csub(e0, o0); v[5] = csub(e1, o1); v[6] = csub(e2, o2); v[7] = csub(e3, o3);
}
DI int fpad(int i) { return i + (i >> 3); }
DI cf ldc(const LAS char* b, int i) { const f32x2 v = *(const LAS f32x2*)(b + i * 8); return {v[0], v[1]}; }
DI void stc(LAS char* b, int i, cf v) { *(LAS f32x2*)(b + i * 8) = (f32x2){v.x, v.y}; }
template <bool INV> DI cf twid(const LAS char* tw, int i) { cf t = ldc(tw, i); if (INV) t.y = -t.y; return t; }
template <bool INV, int NS> DI void pass8(cf (&v)[8], int t, const LAS char* tw, LAS char* dst) {
    const int k = t & (NS - 1);
    if (NS > 1) {
#pragma unroll
        for (int r = 1; r < 8; ++r) v[r] = cmul(v[r], twid<INV>(tw, k * r * (256 / NS)));
    }
    fft8<INV>(v);
    const int j = (t - k) * 8 + k;
#pragma unroll
    for (int r = 0; r < 8; ++r) stc(dst, fpad(j + r * NS), v[r]);
}
template <int STR> DI void gather8(cf (&v)[8], int t, const LAS char* src) {
#pragma unroll
    for (int r = 0; r < 8; ++r) v[r] = ldc(src, fpad(t + r * STR));
}
DI void wsync() { asm volatile("s_waitcnt lgkmcnt(0)" ::: "memory"); }
template <bool INV> DI void fft512(cf (&v)[8], int l, const LAS char* tw, LAS char* buf) {
    pass8<INV, 1>(v, l, tw, buf); wsync(); gather8<64>(v, l, buf); wsync();
    pass8<INV, 8>(v, l, tw, buf); wsync(); gather8<64>(v, l, buf); wsync();
#pragma unroll
    for (int r = 1; r < 8; ++r) v[r] = cmul(v[r], twid<INV>(tw, l * r * 4));
    fft8<INV>(v);
}
template <bool INV> DI void fft2048(cf (&v)[8], int t, const LAS char* tw, LAS char* b0, LAS char* b1) {
    pass8<INV, 1>(v, t, tw, b0); bar(); gather8<256>(v, t, b0);
    pass8<INV, 8>(v, t, tw, b1); bar(); gather8<256>(v, t, b1);
    pass8<INV, 64>(v, t, tw, b0); bar(); gather8<256>(v, t, b0);
#pragma unroll
    for (int u = 0; u < 2; ++u) {
        const int k = t + 256 * u;
#pragma unroll
        for (int r = 1; r < 4; ++r) v[u + 2 * r] = cmul(v[u + 2 * r], twid<INV>(tw, k * r));
        fft4<INV>(v[u], v[u + 2], v[u + 4], v[u + 6]);
    }
    bar();
}
constexpr int LF_TW = 0, LF_T = 16384, LF_W = 84352, LF_B0 = 16384, LF_B1 = 16384 + 18432;
DI float bf2f(unsigned short x) { return __builtin_bit_cast(float, (unsigned)x << 16); }
DI void fft_role(const Params& P, const int j, LAS char* lds) {
    const int tid = threadIdx.x, l = tid & 63, w = __builtin_amdgcn_readfirstlane(tid >> 6);
    const rsrc_t rs = __builtin_amdgcn_make_buffer_rsrc(P.ws, 0, 0x7fffffff, 0x00020000);
    const rsrc_t rc = __builtin_amdgcn_make_buffer_rsrc(P.ctl, 0, CT_BYTES, 0x00020000);
    const LAS char* tw = lds + LF_TW;
    for (int i = tid; i < 2048; i += 256) { float sn, cn; sincospif((float)i * (1.0f / 1024.0f), &sn, &cn); stc(lds + LF_TW, i, cf{cn, -sn}); }
    bar();
    const int s0 = 32 * j;
    LAS char* T = lds + LF_T;
    LAS char* wb = lds + LF_W + w * 4608;
    for (int pi = 4 * w; pi < 4 * w + 4; ++pi) {
        const unsigned short* xa = (const unsigned short*)P.Ax + (size_t)(s0 + 2 * pi) * 512;
        cf v[8];
#pragma unroll
        for (int r = 0; r < 8; ++r) v[r] = cf{bf2f(xa[l + 64 * r]), bf2f(xa[512 + l + 64 * r])};
        fft512<false>(v, l, tw, wb);
#pragma unroll
        for (int r = 0; r < 8; ++r) stc(wb, fpad(l + 64 * r), v[r]);
        wsync();
#pragma unroll
        for (int r = 0; r < 4; ++r) {
            const int k = l + 64 * r;
            const cf a = ldc(wb, fpad(k)), b = ldc(wb, fpad((512 - k) & 511));
            stc(T, k * 33 + 2 * pi, cf{0.5f * (a.x + b.x), 0.5f * (a.y - b.y)});
            stc(T, k * 33 + 2 * pi + 1, cf{0.5f * (a.y + b.y), 0.5f * (b.x - a.x)});
        }
        if (l == 0) { const cf a = ldc(wb, fpad(256)); stc(T, 256 * 33 + 2 * pi, cf{a.x, 0.f}); stc(T, 256 * 33 + 2 * pi + 1, cf{a.y, 0.f}); }
        wsync();
    }
    bar();
    for (int k = tid >> 4; k < 257; k += 16) {
        const int c = tid & 15;
        const cf a = ldc(T, k * 33 + 2 * c), b = ldc(T, k * 33 + 2 * c + 1);
        st128((u32x4){f2u(a.x), f2u(a.y), f2u(b.x), f2u(b.y)}, rs, WS_F + (k * 2048 + s0 + 2 * c) * 8);
    }
    drain();
    bar();
    if (tid == 0) st32(1u, rc, CT_F1 + j * 4);
    wait_flags(rc, CT_F1, 64, l, 4);
    const int ncol = (j == 0) ? 5 : 4;
    for (int ci = 0; ci < ncol; ++ci) {
        const int k = (ci < 4) ? 4 * j + ci : 256;
        cf v[8];
#pragma unroll
        for (int r = 0; r < 8; ++r) { const u32x2 x = ld64(rs, WS_F + (k * 2048 + tid + 256 * r) * 8); v[r] = cf{u2f(x.x), u2f(x.y)}; }
        cf W[8];
#pragma unroll
        for (int r = 0; r < 8; ++r) { const size_t o = (size_t)(tid + 256 * r) * 257 + k; W[r] = cf{bf2f(((const unsigned short*)P.wr)[o]), bf2f(((const unsigned short*)P.wi)[o])}; }
        fft2048<false>(v, tid, tw, lds + LF_B0, lds + LF_B1);
#pragma unroll
        for (int r = 0; r < 8; ++r) v[r] = cmul(v[r], W[r]);
        fft2048<true>(v, tid, tw, lds + LF_B0, lds + LF_B1);
#pragma unroll
        for (int r = 0; r < 8; ++r) st64((u32x2){f2u(v[r].x), f2u(v[r].y)}, rs, WS_F + (k * 2048 + tid + 256 * r) * 8);
    }
    drain();
    bar();
    if (tid == 0) st32(1u, rc, CT_F2 + j * 4);
    wait_flags(rc, CT_F2, 64, l, 4);
    for (int k = tid >> 4; k < 257; k += 16) {
        const int c = tid & 15;
        const u32x4 x = ld128(rs, WS_F + (k * 2048 + s0 + 2 * c) * 8);
        stc(T, k * 33 + 2 * c, cf{u2f(x.x), u2f(x.y)}); stc(T, k * 33 + 2 * c + 1, cf{u2f(x.z), u2f(x.w)});
    }
    bar();
    for (int pi = 4 * w; pi < 4 * w + 4; ++pi) {
        cf v[8];
#pragma unroll
        for (int r = 0; r < 8; ++r) {
            const int kk = l + 64 * r, k = kk <= 256 ? kk : 512 - kk;
            const cf a = ldc(T, k * 33 + 2 * pi), b = ldc(T, k * 33 + 2 * pi + 1);
            const bool edge = (k == 0) | (k == 256);
            const float ay = edge ? 0.f : a.y, by = edge ? 0.f : b.y;
            v[r] = kk <= 256 ? cf{a.x - by, ay + b.x} : cf{a.x + by, b.x - ay};
        }
        fft512<true>(v, l, tw, wb);
        unsigned short* o = (unsigned short*)P.out + (size_t)(s0 + 2 * pi) * 1024;
        constexpr float sc = 1.0f / (2048.0f * 512.0f);
#pragma unroll
        for (int r = 0; r < 8; ++r) {
            const unsigned pk = pack2(v[r].x * sc, v[r].y * sc);
            o[l + 64 * r] = (unsigned short)pk; o[1024 + l + 64 * r] = (unsigned short)(pk >> 16);
        }
        wsync();
    }
}

__global__ void __launch_bounds__(256, 1) gfnet_rmt_kernel(Params P) {
    extern __shared__ __attribute__((aligned(16))) char smem[];
    LAS char* lds = (LAS char*)smem;
    const int b = blockIdx.x, x = b & 7, ix = b >> 3;
    if (x < 4) {
        if (ix < 16) { const int r = x * 16 + ix; chain_role(P, r >> 1, r & 1, lds); }
        else fft_role(P, x * 16 + ix - 16, lds);
    } else {
        if (ix < 16) lead_role(P, (x - 4) * 8 + (ix >> 1), ix & 1, lds);
        else tail_role(P, x - 4, ix - 16, lds);
    }
}

#define DBG_D1 5
#define DBG_D2 10
extern "C" void kernel_launch(void* const* d_in, const int* in_sizes, int n_in, void* d_out, int out_size, void* d_ws, size_t ws_size,
                              hipStream_t stream) {
    (void)in_sizes; (void)n_in; (void)out_size; (void)ws_size;
    Params P;
    P.Ax = d_in[0]; P.wr = d_in[1]; P.wi = d_in[2]; P.Bx = d_in[3]; P.mem0 = d_in[4];
    P.Wq = d_in[5]; P.Wk = d_in[6]; P.Wv = d_in[7]; P.Wo = d_in[8];
    P.out = d_out; P.ctl = (char*)d_ws;
    P.ws = (char*)(((uintptr_t)d_ws + CT_BYTES + (2u << 20) - 1) & ~(uintptr_t)((2u << 20) - 1));
    P.d1 = DBG_D1; P.d2 = DBG_D2; P.par = 0; P.gap = 0;
    (void)hipFuncSetAttribute((const void*)gfnet_rmt_kernel, hipFuncAttributeMaxDynamicSharedMemorySize, LDS_BYTES);
    (void)hipMemsetAsync(d_ws, 0, CT_BYTES, stream);
    gfnet_rmt_kernel<<<256, 256, LDS_BYTES, stream>>>(P);
}
```

```cpp
#include <hip/hip_runtime.h>
#include <hip/hip_bf16.h>
#include <stdint.h>

typedef __bf16 bf16x2 __attribute__((ext_vector_type(2)));
typedef __bf16 bf16x4 __attribute__((ext_vector_type(4)));
typedef __bf16 bf16x8 __attribute__((ext_vector_type(8)));
typedef __bf16 bf16x16 __attribute__((ext_vector_type(16)));
typedef float f32x2 __attribute__((ext_vector_type(2)));
typedef float f32x4 __attribute__((ext_vector_type(4)));
typedef unsigned u32x2 __attribute__((ext_vector_type(2)));
typedef unsigned u32x3 __attribute__((ext_vector_type(3)));
typedef unsigned u32x4 __attribute__((ext_vector_type(4)));
typedef unsigned u32x8 __attribute__((ext_vector_type(8)));
#define LAS __attribute__((address_space(3)))
#define DI __device__ __forceinline__
#define BC(T, x) __builtin_bit_cast(T, x)
typedef __amdgpu_buffer_rsrc_t rsrc_t;

constexpr unsigned SENT = 0xFFFFFFFFu;
constexpr int SC1 = 16;
constexpr int SPIN_CAP = 1 << 20;

constexpr unsigned CT_KV = 0;
constexpr unsigned CT_M = 8192;
constexpr unsigned CT_O = 32768;
constexpr unsigned CT_F1 = 28672;
constexpr unsigned CT_F2 = 28928;
constexpr unsigned CT_R = 29184;
constexpr unsigned CT_L = 30720;
constexpr unsigned CT_CFG = 29440;
constexpr unsigned CT_TK = 29456;
constexpr unsigned CT_BYTES = 40960;
constexpr unsigned WS_RA = 0;
constexpr unsigned WS_RB = 2u << 20;
constexpr unsigned WS_MRING = WS_RA + 64 * 16384;
constexpr unsigned WS_KX = 4u << 20;
constexpr unsigned WS_VX = 20u << 20;
constexpr unsigned WS_OX = 52u << 20;
constexpr unsigned WS_KM = 68u << 20;
constexpr unsigned WS_VM = 76u << 20;
constexpr unsigned WS_F = 80u << 20;
constexpr unsigned WS_DBG = 88u << 20;

struct Params {
    const void* Ax; const void* wr; const void* wi; const void* Bx; const void* mem0;
    const void* Wq; const void* Wk; const void* Wv; const void* Wo;
    void* out; char* ctl; char* ws;
    int d1, d2, gap0, gap1;
};

DI unsigned pack2(float a, float b) { f32x2 v = {a, b}; return BC(unsigned, __builtin_convertvector(v, bf16x2)); }
DI void bar() { asm volatile("s_waitcnt lgkmcnt(0)\n\ts_barrier" ::: "memory"); }
DI void drain() { asm volatile("s_waitcnt vmcnt(0)" ::: "memory"); }
DI void dma16(const void* gsrc, unsigned lds_dst) {
    unsigned keep;
    asm volatile("s_mov_b32 %0, m0\n\ts_mov_b32 m0, %2\n\ts_nop 0\n\tglobal_load_lds_dwordx4 %1, off\n\ts_mov_b32 m0, %0" : "=&s"(keep) : "v"(gsrc), "s"(lds_dst) : "memory");
}
DI void dma16c(const void* gsrc, unsigned lds_dst) {
    unsigned keep;
    asm volatile("s_mov_b32 %0, m0\n\ts_mov_b32 m0, %2\n\ts_nop 0\n\tglobal_load_lds_dwordx4 %1, off sc1\n\ts_mov_b32 m0, %0" : "=&s"(keep) : "v"(gsrc), "s"(lds_dst) : "memory");
}
DI u32x2 trd(const LAS char* p) { return BC(u32x2, __builtin_amdgcn_ds_read_tr16_b64_v4bf16((LAS bf16x4*)p)); }
DI u32x4 lds128(const LAS char* p) { return *(const LAS u32x4*)p; }
DI f32x4 smf(u32x4 a, const u32x8& b, f32x4 c, int idx) { return __builtin_amdgcn_smfmac_f32_16x16x64_bf16(BC(bf16x8, a), BC(bf16x16, b), c, idx, 0, 0); }
DI f32x4 mf32(u32x4 a, u32x4 b, f32x4 c) { return __builtin_amdgcn_mfma_f32_16x16x32_bf16(BC(bf16x8, a), BC(bf16x8, b), c, 0, 0, 0); }
DI void sleepn(int n) { for (int i = 0; i < n; ++i) __builtin_amdgcn_s_sleep(1); }
DI unsigned opq(unsigned x) { asm volatile("" : "+v"(x)); return x; }
DI float opqf(float x) { asm volatile("" : "+v"(x)); return x; }
DI f32x4 zero4() { f32x4 z = {0, 0, 0, 0}; asm volatile("" : "+v"(z)); return z; }
#define SB() __builtin_amdgcn_sched_barrier(0)
#define PIN1(x) asm volatile("" : "+v"(x))
#define PIN2(x, y) asm volatile("" : "+v"(x), "+v"(y))
#define PINA1(x) asm volatile("" : "+a"(x))
#define PINA2(x, y) asm volatile("" : "+a"(x), "+a"(y))
DI float vmax(float a, float b) { float r; asm("v_max_f32 %0, %1, %2" : "=v"(r) : "v"(a), "v"(b)); return r; }
DI float vmax3(float a, float b, float c) { float r; asm("v_max3_f32 %0, %1, %2, %3" : "=v"(r) : "v"(a), "v"(b), "v"(c)); return r; }
DI float u2f(unsigned x) { return __builtin_bit_cast(float, x); }
DI unsigned f2u(float x) { return __builtin_bit_cast(unsigned, x); }
DI float xmax16(float x) { unsigned a = f2u(x); auto r = __builtin_amdgcn_permlane16_swap(a, opq(a), false, false); return vmax(u2f(r[0]), u2f(r[1])); }
DI float xmax32(float x) { unsigned a = f2u(x); auto r = __builtin_amdgcn_permlane32_swap(a, opq(a), false, false); return vmax(u2f(r[0]), u2f(r[1])); }
DI float xadd16(float x) { unsigned a = f2u(x); auto r = __builtin_amdgcn_permlane16_swap(a, opq(a), false, false); return u2f(r[0]) + u2f(r[1]); }
DI float xadd32(float x) { unsigned a = f2u(x); auto r = __builtin_amdgcn_permlane32_swap(a, opq(a), false, false); return u2f(r[0]) + u2f(r[1]); }
DI unsigned umax3(unsigned a, unsigned b, unsigned c) { return __builtin_elementwise_max(__builtin_elementwise_max(a, b), c); }
DI unsigned smax(unsigned acc, const u32x4& v) { return umax3(umax3(acc, v.x, v.y), v.z, v.w); }
DI unsigned fold(const f32x4& c) { return pack2(opqf(c[0] + c[1]), c[2] + c[3]); }
DI u32x4 ld128(rsrc_t rs, unsigned off) { return __builtin_amdgcn_raw_buffer_load_b128(rs, off, 0, SC1); }
DI u32x2 ld64(rsrc_t rs, unsigned off) { return __builtin_amdgcn_raw_buffer_load_b64(rs, off, 0, SC1); }
DI unsigned ld32(rsrc_t rs, unsigned off) { return __builtin_amdgcn_raw_buffer_load_b32(rs, off, 0, SC1); }
DI void st128(u32x4 v, rsrc_t rs, unsigned off) { __builtin_amdgcn_raw_buffer_store_b128(v, rs, off, 0, SC1); }
DI void st64(u32x2 v, rsrc_t rs, unsigned off) { __builtin_amdgcn_raw_buffer_store_b64(v, rs, off, 0, SC1); }
DI void st32(unsigned v, rsrc_t rs, unsigned off) { __builtin_amdgcn_raw_buffer_store_b32(v, rs, off, 0, SC1); }
DI void wait_flags(rsrc_t rc, unsigned off, int n, int l, int nap) {
    const unsigned o = off + (l < n ? l : 0) * 4;
    for (int spins = 0; spins < SPIN_CAP; ++spins) {
        const unsigned v = ld32(rc, o);
        if (!__any(v == 0)) break;
        for (int i = 0; i < nap; ++i) __builtin_amdgcn_s_sleep(1);
        asm volatile("" ::: "memory");
    }
    asm volatile("" ::: "memory");
}
template <int C> struct IC { static constexpr int v = C; };
template <int I, int N, typename F> DI void static_for(F&& f) { if constexpr (I < N) { f(IC<I>{}); static_for<I + 1, N>(f); } }
#define WS_WIN 8
template <int NCH, int WIN, typename Src, typename Wr, typename Use> DI void weight_stream(LAS char* slots, Src&& src, Wr&& wr, Use&& use, unsigned long long* dbg = nullptr) {
    u32x4 raw[NCH][4];
    auto issue = [&](auto cc) {
        constexpr int c = decltype(cc)::v;
#pragma unroll
        for (int i = 0; i < 4; ++i) raw[c][i] = *(const u32x4*)src(cc, i);
    };
    static_for<0, (WIN < NCH ? WIN : NCH)>(issue);
    asm volatile("" ::: "memory");
    static_for<0, NCH>([&](auto cc) {
        constexpr int c = decltype(cc)::v;
        LAS char* slot = slots + (c & 1) * 16384;
#pragma unroll
        for (int i = 0; i < 4; ++i) *(LAS u32x4*)(slot + wr(cc, i)) = raw[c][i];
        if constexpr (c + WIN < NCH) issue(IC<c + WIN>{});
        bar();
        use(cc, (const LAS char*)slot);
        asm volatile("" ::: "memory");
    });
}
constexpr float CS = 0.08838834764831845f * 1.4426950408889634f;

constexpr int L_STG = 0;
constexpr int L_MI = 131072;
constexpr int L_KV = 0;
constexpr int L_ZQ = L_MI + 8192;
constexpr int L_ZK = L_ZQ + 2048;
constexpr int L_O = L_ZK + 4096;
constexpr int L_END = L_O + 1024;
constexpr int LDS_BYTES = L_END;
#define STAMP(i)

DI void chain_role(const Params& P, const int h, const int hv, LAS char* lds) {
    const int tid = threadIdx.x, l = tid & 63, w = __builtin_amdgcn_readfirstlane(tid >> 6);
    const int n = l & 15, g = l >> 4;
    const int rank = 2 * h + hv;
    const rsrc_t rs = __builtin_amdgcn_make_buffer_rsrc(P.ws, 0, 0x7fffffff, 0x00020000);
    const rsrc_t rc = __builtin_amdgcn_make_buffer_rsrc(P.ctl, 0, CT_BYTES, 0x00020000);
    const unsigned ldsbase = (unsigned)(size_t)lds;
    const u32x4 s4 = {SENT, SENT, SENT, SENT};
    u32x2 m0a[2], m0b[2];
#pragma unroll
    for (int k = 0; k < 2; ++k) {
        const int ch = tid + 256 * k, cg = ch >> 4, rho = ch & 15, i = rho >> 1, hf = rho & 1;
        const char* src = (const char*)P.mem0 + i * 1024 + (64 * (cg >> 2) + 32 * hf + 4 * (cg & 3)) * 2;
        m0a[k] = *(const u32x2*)src; m0b[k] = *(const u32x2*)(src + 32);
    }

    u32x8 Wq[2][8], Wk[2][8], Wv[8], Wo[8];
    {
        const int r16 = 16 * w + (l >> 4), u16 = l & 15;
        const int r8 = 32 * w + (l >> 3), u8 = l & 7;
        const char* sq = (const char*)P.Wq + (size_t)r16 * 8192 + h * 256 + u16 * 16;
        const char* sk = (const char*)P.Wk + (size_t)r16 * 8192 + h * 256 + u16 * 16;
        const char* sv = (const char*)P.Wv + (size_t)r8 * 8192 + h * 256 + hv * 128 + u8 * 16;
        const char* so = (const char*)P.Wo + (size_t)(h * 128 + 64 * hv + r16) * 1024 + u16 * 16;
        auto dsw = [](int r) { return (r & 1) | ((r >> 3) & 6); };
        auto esw = [](int r) { return (r & 1) | ((r >> 1) & 6); };
        auto vsw = [](int r) { return (r >> 4) & 3; };
        const int qq = (l >> 2) & 3, p = l & 3;
        const int drow = 32 * (qq >> 1) + 16 * (g & 1) + 4 * (g >> 1) + (qq & 1);
        const int erow = 16 * (g >> 1) + 8 * (qq >> 1) + 4 * (g & 1) + (qq & 1);
        auto frag = [&](const LAS char* slot, int row, int sw, int seg, int stride, int du0, int du1) {
            const LAS char* a = slot + row * stride + ((seg ^ sw) * 32) + 8 * p;
            const u32x2 r0 = trd(a), r1 = trd(a + du0 * stride), r2 = trd(a + du1 * stride), r3 = trd(a + (du0 + du1) * stride);
            const u32x8 f = {r0.x, r0.y, r1.x, r1.y, r2.x, r2.y, r3.x, r3.y};
            return f;
        };
        weight_stream<24, WS_WIN>(lds + L_STG,
            [&](auto cc, int i) -> const char* {
                constexpr int c = decltype(cc)::v;
                if constexpr (c < 16) return (c < 8 ? sq : sk) + (size_t)(64 * (c & 7) + 4 * i) * 8192;
                else if constexpr (c < 20) return sv + (size_t)(128 * (c - 16) + 8 * i) * 8192;
                else return so + (c - 20) * 256 + (size_t)(4 * i) * 1024;
            },
            [&](auto cc, int i) -> int {
                constexpr int c = decltype(cc)::v;
                if constexpr (c < 16) { const int r = r16 + 4 * i; return r * 256 + (((u16 >> 1) ^ dsw(r)) * 32) + (u16 & 1) * 16; }
                else if constexpr (c < 20) { const int r = r8 + 8 * i; return r * 128 + (((u8 >> 1) ^ vsw(r)) * 32) + (u8 & 1) * 16; }
                else { const int r = r16 + 4 * i; return r * 256 + (((u16 >> 1) ^ esw(r)) * 32) + (u16 & 1) * 16; }
            },
            [&](auto cc, const LAS char* slot) {
                constexpr int c = decltype(cc)::v;
                if constexpr (c == 1) {
#pragma unroll
                    for (int i = 0; i < 4; ++i) { st128(s4, rs, WS_RA + rank * 16384 + tid * 16 + i * 4096); st128(s4, rs, WS_RB + rank * 16384 + tid * 16 + i * 4096); }
                    if (tid < 64) st128(s4, rs, WS_MRING + (tid >> 3) * 8192 + rank * 128 + (tid & 7) * 16);
                }
                if constexpr (c == 2 + WS_WIN) { if (tid == 0) st32(1u, rc, CT_R + rank * 4); }
                if constexpr (c < 8) { Wq[0][c] = frag(slot, drow, dsw(drow), 2 * w, 256, 2, 8); Wq[1][c] = frag(slot, drow, dsw(drow), 2 * w + 1, 256, 2, 8); }
                else if constexpr (c < 16) { Wk[0][c - 8] = frag(slot, drow, dsw(drow), 2 * w, 256, 2, 8); Wk[1][c - 8] = frag(slot, drow, dsw(drow), 2 * w + 1, 256, 2, 8); }
                else if constexpr (c < 20) { Wv[2 * (c - 16)] = frag(slot, drow, vsw(drow), w, 128, 2, 8); Wv[2 * (c - 16) + 1] = frag(slot + 64 * 128, drow, vsw(drow), w, 128, 2, 8); }
                else if (w == c - 20) {
#pragma unroll
                    for (int t = 0; t < 8; ++t) Wo[t] = frag(slot, erow, esw(erow), t, 256, 2, 32);
                }
            }
            );
    }
    if (tid == 0) st32(1u, rc, CT_L + rank * 4);
#pragma unroll
    for (int k = 0; k < 2; ++k) *(LAS u32x4*)(lds + L_MI + (tid + 256 * k) * 16) = (u32x4){m0a[k].x, m0a[k].y, m0b[k].x, m0b[k].y};
    *(LAS u32x4*)(lds + L_ZK + tid * 16) = (u32x4){0, 0, 0, 0};
    wait_flags(rc, CT_R, 64, l, 1);
    const unsigned parA = __builtin_amdgcn_readfirstlane(ld32(rc, CT_CFG)) & 8192u, parB = __builtin_amdgcn_readfirstlane(ld32(rc, CT_CFG + 4)) & 8192u;
    const unsigned myin0 = WS_RA + parA + rank * 16384;
    const unsigned slotd = (WS_RB + parB) - (WS_RA + parA);
    const unsigned mring = WS_MRING + parA;
    unsigned long long kvmask = 0;
    auto need_kv = [&](int s) {
        for (int spins = 0; !((kvmask >> s) & 1) && spins < SPIN_CAP; ++spins) {
            kvmask = __ballot(ld32(rc, CT_KV + h * 256 + l * 4) != 0);
            asm volatile("" ::: "memory");
        }
    };
    const char* kvsrc = P.ws + (w == 3 ? WS_VX + h * 8192 + hv * 4096 : WS_KX + h * 8192 + (w - 1) * 4096) + l * 16;
    auto fetch_kv = [&](int s) {
        if (w != 0) {
            need_kv(s);
            const unsigned dst = __builtin_amdgcn_readfirstlane(ldsbase + L_KV + (s & 1) * 12288 + (w - 1) * 4096);
#pragma unroll
            for (int i = 0; i < 4; ++i) dma16c(kvsrc + (size_t)s * (32 * 8192) + i * 1024, dst + i * 1024);
        }
    };
    bar();
    fetch_kv(0);
    drain();
    const int idx = (n & 1) ? 0xEEEE : 0x4444;
    const int qq = (l >> 2) & 3, p4 = l & 3;
    const LAS char* aMI = lds + L_MI + l * 16;
    LAS char* wZQ = lds + L_ZQ + (32 * w + n) * 16 + 4 * g;
    LAS char* wZK = lds + L_ZK + (32 * w + n) * 32 + 8 * g;
    const LAS char* rZQ = lds + L_ZQ + (4 * g + qq) * 16 + (p4 & 1) * 8;
    const LAS char* rZK = lds + L_ZK + (4 * g + qq) * 32 + p4 * 8;
    const LAS char* rZKp = rZK + (2 * hv + (w >> 1)) * 1024 + (w & 1) * 512;
    LAS char* wO = lds + L_O + (n & 7) * 128 + (16 * w + 4 * g) * 2;
    const LAS char* rO = lds + L_O + (n >> 1) * 128 + g * 32 + (n & 1) * 16;
    const unsigned inb0 = WS_RA + parA + (2 * (8 * w + (n >> 2)) + (g >> 1)) * 16384 + rank * 128 + ((g & 1) * 4 + (n & 3)) * 16;
    const unsigned kmo = WS_KM + h * 4096 + (2 * hv + (w >> 1)) * 1024 + (w & 1) * 512 + l * 8;
    const unsigned vmo = WS_VM + h * 2048 + (4 * hv + w) * 256 + l * 4;
    const u32x4 sel = {n == 0 ? 0x3F80u : n == 1 ? 0x3F800000u : 0u, n == 2 ? 0x3F80u : n == 3 ? 0x3F800000u : 0u,
                       n == 4 ? 0x3F80u : n == 5 ? 0x3F800000u : 0u, n == 6 ? 0x3F80u : n == 7 ? 0x3F800000u : 0u};
    const unsigned pubo = (rank >> 1) * 256 + (2 * (4 * (rank & 1) + 2 * g + (n & 1)) + (n >> 2)) * 16 + ((n >> 1) & 1) * 8;
    bar();
    for (int s = 0;; ++s) {
        STAMP(11);
        const LAS char* aKX = lds + L_KV + (s & 1) * 12288 + l * 16;
        u32x4 a[8];
#pragma unroll
        for (int ks = 0; ks < 8; ++ks) a[ks] = lds128(aMI + ks * 1024);
        f32x4 q0 = zero4(), q1 = zero4(), k0 = zero4(), k1 = zero4();
        SB();
#pragma unroll
        for (int ks = 0; ks < 8; ++ks) { q0 = smf(a[ks], Wq[0][ks], q0, idx); q1 = smf(a[ks], Wq[1][ks], q1, idx); }
        PIN2(q0, q1); SB();
        STAMP(7);
        k0 = smf(a[0], Wk[0][0], k0, idx); k1 = smf(a[0], Wk[1][0], k1, idx);
        PIN2(k0, k1); SB();
        { const unsigned z0 = fold(q0); *(LAS unsigned*)(wZQ) = z0; }
        k0 = smf(a[1], Wk[0][1], k0, idx);
        PIN1(k0); SB();
        { const unsigned z1 = fold(q1); *(LAS unsigned*)(wZQ + 256) = z1; }
        k1 = smf(a[1], Wk[1][1], k1, idx);
        PIN1(k1); SB();
        k0 = smf(a[2], Wk[0][2], k0, idx); k1 = smf(a[2], Wk[1][2], k1, idx);
        PIN2(k0, k1); SB();
        bar();
        STAMP(0);
        u32x4 qb[4];
#pragma unroll
        for (int kk = 0; kk < 4; ++kk) { const u32x2 x = trd(rZQ + kk * 512), y = trd(rZQ + kk * 512 + 256); qb[kk] = (u32x4){x.x, x.y, y.x, y.y}; }
        SB();
        k0 = smf(a[3], Wk[0][3], k0, idx); k1 = smf(a[3], Wk[1][3], k1, idx);
        PIN2(k0, k1); SB();
        u32x4 kx0[4], kx1[4];
        kx0[0] = lds128(aKX); kx1[0] = lds128(aKX + 4096);
        kx0[1] = lds128(aKX + 1024); kx1[1] = lds128(aKX + 4096 + 1024);
        SB();
        k0 = smf(a[4], Wk[0][4], k0, idx); k1 = smf(a[4], Wk[1][4], k1, idx);
        k0 = smf(a[5], Wk[0][5], k0, idx); k1 = smf(a[5], Wk[1][5], k1, idx);
        PIN2(k0, k1); SB();
        kx0[2] = lds128(aKX + 2048); kx1[2] = lds128(aKX + 4096 + 2048);
        kx0[3] = lds128(aKX + 3072); kx1[3] = lds128(aKX + 4096 + 3072);
        SB();
        k0 = smf(a[6], Wk[0][6], k0, idx); k1 = smf(a[6], Wk[1][6], k1, idx);
        k0 = smf(a[7], Wk[0][7], k0, idx); k1 = smf(a[7], Wk[1][7], k1, idx);
        PIN2(k0, k1); SB();
        f32x4 S0 = {0, 0, 0, 0}, S1 = S0, S2 = S0;
        S0 = mf32(kx0[0], qb[0], S0); S1 = mf32(kx1[0], qb[0], S1);
        PINA2(S0, S1); SB();
        { const unsigned z0 = fold(k0); *(LAS unsigned*)(wZK) = z0; }
        S0 = mf32(kx0[1], qb[1], S0);
        PINA1(S0); SB();
        { const unsigned z1 = fold(k1); *(LAS unsigned*)(wZK + 512) = z1; }
        S1 = mf32(kx1[1], qb[1], S1);
        PINA1(S1); SB();
        S0 = mf32(kx0[2], qb[2], S0); S1 = mf32(kx1[2], qb[2], S1);
        PINA2(S0, S1); SB();
        bar();
        STAMP(1);
        u32x4 km[4];
#pragma unroll
        for (int kk = 0; kk < 2; ++kk) { const u32x2 x = trd(rZK + kk * 1024), y = trd(rZK + kk * 1024 + 512); km[kk] = (u32x4){x.x, x.y, y.x, y.y}; }
        SB();
        S0 = mf32(kx0[3], qb[3], S0);
        PINA1(S0); SB();
#pragma unroll
        for (int kk = 2; kk < 4; ++kk) { const u32x2 x = trd(rZK + kk * 1024), y = trd(rZK + kk * 1024 + 512); km[kk] = (u32x4){x.x, x.y, y.x, y.y}; }
        SB();
        S1 = mf32(kx1[3], qb[3], S1);
        PINA1(S1); SB();
        a[0] = lds128(aMI); a[1] = lds128(aMI + 1024); a[2] = lds128(aMI + 2048); a[3] = lds128(aMI + 3072);
        f32x4 v0 = zero4();
        SB();
        float m = vmax(vmax3(vmax3(S0[0], S0[1], S0[2]), S0[3], S1[0]), vmax3(S1[1], S1[2], S1[3]));
        PIN1(m); SB();
        S2 = mf32(km[0], qb[0], S2); S2 = mf32(km[1], qb[1], S2);
        PINA1(S2); SB();
        a[4] = lds128(aMI + 4096); a[5] = lds128(aMI + 5120);
        SB();
        S2 = mf32(km[2], qb[2], S2); S2 = mf32(km[3], qb[3], S2);
        PINA1(S2); SB();
        STAMP(8);
        a[6] = lds128(aMI + 6144); a[7] = lds128(aMI + 7168);
        const u32x2 kmp = trd(rZKp);
        const u32x4 vx = lds128(aKX + 8192 + w * 1024);
        SB();
        v0 = smf(a[0], Wv[0], v0, idx);
        PIN1(v0); SB();
        v0 = smf(a[1], Wv[1], v0, idx);
        PIN1(v0); SB();
        m = vmax3(m, S2[0], S2[1]);
        m = xmax16(m);
        PIN1(m); SB();
        v0 = smf(a[2], Wv[2], v0, idx);
        PIN1(v0); SB();
        m = xmax32(m);
        float mc = m * CS;
        PIN1(mc); SB();
        float p0[4], p1[4], p2[2];
        v0 = smf(a[3], Wv[3], v0, idx);
        PIN1(v0); SB();
        p0[0] = __builtin_amdgcn_exp2f(S0[0] * CS - mc); p0[1] = __builtin_amdgcn_exp2f(S0[1] * CS - mc);
        PIN2(p0[0], p0[1]); SB();
        v0 = smf(a[4], Wv[4], v0, idx);
        PIN1(v0); SB();
        p0[2] = __builtin_amdgcn_exp2f(S0[2] * CS - mc); p0[3] = __builtin_amdgcn_exp2f(S0[3] * CS - mc);
        PIN2(p0[2], p0[3]); SB();
        v0 = smf(a[5], Wv[5], v0, idx);
        PIN1(v0); SB();
        p1[0] = __builtin_amdgcn_exp2f(S1[0] * CS - mc); p1[1] = __builtin_amdgcn_exp2f(S1[1] * CS - mc);
        PIN2(p1[0], p1[1]); SB();
        v0 = smf(a[6], Wv[6], v0, idx);
        PIN1(v0); SB();
        p1[2] = __builtin_amdgcn_exp2f(S1[2] * CS - mc); p1[3] = __builtin_amdgcn_exp2f(S1[3] * CS - mc);
        PIN2(p1[2], p1[3]); SB();
        v0 = smf(a[7], Wv[7], v0, idx);
        PIN1(v0); SB();
        p2[0] = __builtin_amdgcn_exp2f(S2[0] * CS - mc); p2[1] = __builtin_amdgcn_exp2f(S2[1] * CS - mc);
        u32x4 pb = {pack2(p0[0], p0[1]), pack2(p0[2], p0[3]), pack2(p1[0], p1[1]), pack2(p1[2], p1[3])};
        PIN1(pb); SB();
        STAMP(9);
        f32x4 ot = {0, 0, 0, 0};
        ot = mf32(vx, pb, ot);
        PINA1(ot); SB();
        const unsigned vmd = fold(v0);
        const u32x4 pb2 = {pack2(p2[0], p2[1]), 0u, 0u, 0u};
        float ls = ((p0[0] + p0[1]) + (p0[2] + p0[3])) + ((p1[0] + p1[1]) + (p1[2] + p1[3]));
        ot = mf32((u32x4){vmd, 0u, 0u, 0u}, pb2, ot);
        PINA1(ot); SB();
        ls += p2[0] + p2[1];
        ls = xadd32(xadd16(ls));
        const float il = __builtin_amdgcn_rcpf(ls);
        SB();
        *(LAS u32x2*)(wO) = (u32x2){pack2(opqf(ot[0] * il), ot[1] * il), pack2(opqf(ot[2] * il), ot[3] * il)};
        SB();
        if (s == 63) {
            st64(kmp, rs, kmo + s * (32 * 4096));
            st32(vmd, rs, vmo + s * (32 * 2048));
            break;
        }
        bar();
        STAMP(2);
        const u32x4 oa = lds128(rO);
        SB();
        f32x4 c[8];
#pragma unroll
        for (int t = 0; t < 8; ++t) c[t] = zero4();
        unsigned d[8];
        SB();
        c[0] = smf(oa, Wo[0], c[0], idx); c[1] = smf(oa, Wo[1], c[1], idx); c[2] = smf(oa, Wo[2], c[2], idx);
        PIN2(c[0], c[1]); PIN1(c[2]); SB();
#pragma unroll
        for (int t = 0; t < 5; ++t) { c[t + 3] = smf(oa, Wo[t + 3], c[t + 3], idx); PIN1(c[t + 3]); SB(); d[t] = fold(c[t]); PIN1(d[t]); SB(); }
        const unsigned slot = (s & 1) ? slotd : 0u;
        st128((u32x4){d[0], d[1], d[2], d[3]}, rs, inb0 + slot);
        d[5] = fold(c[5]); d[6] = fold(c[6]); d[7] = fold(c[7]);
        st128((u32x4){d[4], d[5], d[6], d[7]}, rs, inb0 + slot + 8 * 16384);
        SB();
        STAMP(3);
        st64(kmp, rs, kmo + s * (32 * 4096));
        st32(vmd, rs, vmo + s * (32 * 2048));
        if (s > 0 && tid == 64) st32(1u, rc, CT_M + ((s - 1) * 64 + rank) * 4);
        fetch_kv(s + 1);
        const unsigned mnext = mring + ((s + 1) & 3) * 16384;
        SB();
        if (w == 0) sleepn(P.d2);
        STAMP(10);
        if (w == 0) {
            const unsigned gb = myin0 + slot + l * 16;
            u32x4 x[8];
#pragma unroll
            for (int k = 0; k < 8; ++k) x[k] = __builtin_amdgcn_raw_buffer_load_b128(rs, gb + (k & 3) * 1024, (k >> 2) * 4096, SC1);
            f32x4 R = {0, 0, 0, 0};
            unsigned mx = 0;
#pragma unroll
            for (int k = 0; k < 8; ++k) { R = mf32(x[k], sel, R); mx = smax(mx, x[k]); }
            if (__any(mx == SENT)) {
                unsigned bad = 0xff;
                for (int spins = 0; bad && spins < SPIN_CAP; ++spins) {
                    unsigned nb = 0;
#pragma unroll
                    for (int k = 0; k < 8; ++k) if (__any(smax(0u, x[k]) == SENT)) nb |= 1u << k;
                    bad = nb;
#pragma unroll
                    for (int k = 0; k < 8; ++k) if ((bad >> k) & 1) x[k] = __builtin_amdgcn_raw_buffer_load_b128(rs, gb + (k & 3) * 1024, (k >> 2) * 4096, SC1);
                    asm volatile("" ::: "memory");
                }
                R = (f32x4){0, 0, 0, 0};
#pragma unroll
                for (int k = 0; k < 8; ++k) R = mf32(x[k], sel, R);
            }
            auto a02 = __builtin_amdgcn_permlane32_swap(f2u(R[0]), f2u(R[2]), false, false);
            auto a13 = __builtin_amdgcn_permlane32_swap(f2u(R[1]), f2u(R[3]), false, false);
            const unsigned dd = pack2(opqf(u2f(a02[0]) + u2f(a02[1])), u2f(a13[0]) + u2f(a13[1]));
            auto de = __builtin_amdgcn_permlane32_swap(dd, opq(dd), false, false);
            if (l < 32 && n < 8) st64((u32x2){de[0], de[1]}, rs, mnext + pubo);
        }
        STAMP(4);
        bar();
        STAMP(5);
        {
            const unsigned b = myin0 + slot + w * 2048 + l * 16;
            st128(s4, rs, b); st128(s4, rs, b + 1024);
            if (w == 0 && s > 0 && l < 8) st128(s4, rs, mring + (s & 3) * 16384 + rank * 128 + l * 16);
        }
        sleepn(P.d1);
        {
            u32x4 m0, m1; int spins = 0;
            for (;;) {
                m0 = ld128(rs, mnext + w * 2048 + l * 16);
                m1 = ld128(rs, mnext + w * 2048 + 1024 + l * 16);
                if (!__any(smax(smax(0u, m0), m1) == SENT)) break;
                if (++spins > SPIN_CAP) break;
                asm volatile("" ::: "memory");
            }
            *(LAS u32x4*)(lds + L_MI + w * 2048 + l * 16) = m0; *(LAS u32x4*)(lds + L_MI + w * 2048 + 1024 + l * 16) = m1;
        }
        bar();
        STAMP(6);
    }
    drain();
    bar();
    if (tid == 0) { st32(1u, rc, CT_M + (62 * 64 + rank) * 4); st32(1u, rc, CT_M + (63 * 64 + rank) * 4); }
}

struct XStage {
    const char* xs; unsigned ldsbase; int w;
    DI void issue1(int s, int buf, int i) const {
        dma16(xs + (size_t)s * 32768 + i * 64, __builtin_amdgcn_readfirstlane(ldsbase + buf * 32768 + w * 8192) + i * 1024);
    }
    DI void issue(int s, int buf) const {
#pragma unroll
        for (int i = 0; i < 8; ++i) issue1(s, buf, i);
    }
};
DI XStage make_xstage(const Params& P, int l, int w, unsigned ldsbase) {
    return XStage{(const char*)P.Bx + (size_t)(16 * (w >> 1) + (l & 15)) * 1024 + ((w & 1) * 8 * 32 + 8 * (l >> 4)) * 2, ldsbase, w};
}
template <typename F, typename D> DI void x_stream(const LAS char* xb, F&& f, D&& dma) {
    u32x4 xa[2][8];
#pragma unroll
    for (int j = 0; j < 8; ++j) xa[0][j] = lds128(xb + (16 * (j & 1) + (j >> 1)) * 1024);
    static_for<0, 4>([&](auto bb) {
        constexpr int b = decltype(bb)::v;
        if constexpr (b < 3) {
#pragma unroll
            for (int j = 0; j < 8; ++j) xa[(b + 1) & 1][j] = lds128(xb + (16 * (j & 1) + 4 * (b + 1) + (j >> 1)) * 1024);
        }
        SB();
#pragma unroll
        for (int j = 0; j < 4; ++j) f(4 * b + (j >> 1), j & 1, xa[b & 1][j]);
        SB();
        dma(2 * b);
        SB();
#pragma unroll
        for (int j = 4; j < 8; ++j) f(4 * b + (j >> 1), j & 1, xa[b & 1][j]);
        SB();
        dma(2 * b + 1);
        SB();
    });
}
template <int NM, typename Put> DI void load_proj_weights(const char* const (&mat)[NM], int h, int l, int w, LAS char* lds, Put&& put) {
    const int g = l >> 4, r16 = 16 * w + (l >> 4), u16 = l & 15;
    const size_t lo = (size_t)r16 * 8192 + h * 256 + u16 * 16;
    auto rsw = [](int r) { return (r & 3) | ((r >> 1) & 4); };
    const int row = 8 * g + ((l >> 2) & 3);
    weight_stream<8 * NM, WS_WIN>(lds + L_STG,
        [&](auto cc, int i) -> const char* { constexpr int ch = decltype(cc)::v; return mat[ch >> 3] + lo + (size_t)(64 * (ch & 7) + 4 * i) * 8192; },
        [&](auto, int i) -> int { const int r = r16 + 4 * i; return r * 256 + (((u16 >> 1) ^ rsw(r)) * 32) + (u16 & 1) * 16; },
        [&](auto cc, const LAS char* slot) {
            constexpr int ch = decltype(cc)::v;
#pragma unroll
            for (int kdl = 0; kdl < 2; ++kdl)
#pragma unroll
                for (int tb = 0; tb < 2; ++tb) {
                    const LAS char* a = slot + (row + 32 * kdl) * 256 + (((2 * w + tb) ^ rsw(row)) * 32) + (l & 3) * 8;
                    const u32x2 r0 = trd(a), r1 = trd(a + 4 * 256);
                    put(IC<(ch >> 3)>{}, tb, 2 * (ch & 7) + kdl, (u32x4){r0.x, r0.y, r1.x, r1.y});
                }
        });
}

DI void kv_role(const Params& P, const int h, LAS char* lds) {
    const int tid = threadIdx.x, l = tid & 63, w = __builtin_amdgcn_readfirstlane(tid >> 6);
    const rsrc_t rs = __builtin_amdgcn_make_buffer_rsrc(P.ws, 0, 0x7fffffff, 0x00020000);
    const rsrc_t rc = __builtin_amdgcn_make_buffer_rsrc(P.ctl, 0, CT_BYTES, 0x00020000);
    const unsigned ldsbase = (unsigned)(size_t)lds;
    u32x4 WK[2][16], WV[2][16];
    {
        const char* const mats[2] = {(const char*)P.Wk, (const char*)P.Wv};
        load_proj_weights<2>(mats, h, l, w, lds, [&](auto m, int tb, int kd, u32x4 f) { if constexpr (decltype(m)::v == 0) WK[tb][kd] = f; else WV[tb][kd] = f; });
    }
    bar();
    const XStage xst = make_xstage(P, l, w, ldsbase);
    xst.issue(0, 0);
    xst.issue(1, 1);
    int buf = 0;
    for (int s = 0; s < 64; ++s) {
        if (s == 0) asm volatile("s_waitcnt vmcnt(8)" ::: "memory"); else if (s == 1) drain();
        bar();
        if (s == 1 && tid == 0) st32(1u, rc, CT_KV + (h * 64) * 4);
        const LAS char* xb = lds + buf * 32768 + l * 16;
        const int bnext = buf == 0 ? 2 : buf - 1;
        f32x4 K[2][2], V[2][2];
#pragma unroll
        for (int i = 0; i < 2; ++i)
#pragma unroll
            for (int j = 0; j < 2; ++j) { K[i][j] = (f32x4){0, 0, 0, 0}; V[i][j] = K[i][j]; }
        x_stream(xb, [&](int kd, int T, const u32x4& xf) {
#pragma unroll
            for (int tb = 0; tb < 2; ++tb) {
                K[tb][T] = mf32(WK[tb][kd], xf, K[tb][T]);
                V[tb][T] = mf32(xf, WV[tb][kd], V[tb][T]);
            }
        }, [&](int i) { if (s < 62) xst.issue1(s + 2, bnext, i); });
        buf = buf == 2 ? 0 : buf + 1;
        if (s > 1) {
            if (s < 62) asm volatile("s_waitcnt vmcnt(8)" ::: "memory"); else drain();
            bar();
            if (tid == 0) st32(1u, rc, CT_KV + (h * 64 + s - 1) * 4);
        }
        const unsigned blk = (s * 32 + h) * 8192 + l * 16;
#pragma unroll
        for (int T = 0; T < 2; ++T)
            st128((u32x4){pack2(K[0][T][0], K[0][T][1]), pack2(K[0][T][2], K[0][T][3]), pack2(K[1][T][0], K[1][T][1]), pack2(K[1][T][2], K[1][T][3])}, rs, WS_KX + blk + (4 * T + w) * 1024);
#pragma unroll
        for (int tb = 0; tb < 2; ++tb)
            st128((u32x4){pack2(V[tb][0][0], V[tb][0][1]), pack2(V[tb][0][2], V[tb][0][3]), pack2(V[tb][1][0], V[tb][1][1]), pack2(V[tb][1][2], V[tb][1][3])}, rs, WS_VX + blk + (2 * w + tb) * 1024);
    }
    drain();
    bar();
    if (tid == 0) st32(1u, rc, CT_KV + (h * 64 + 63) * 4);
}

constexpr int L_QL = 98304;
DI void qa_role(const Params& P, const int h, const int c, LAS char* lds) {
    const int tid = threadIdx.x, l = tid & 63, w = __builtin_amdgcn_readfirstlane(tid >> 6);
    const int n = l & 15, g = l >> 4;
    const rsrc_t rs = __builtin_amdgcn_make_buffer_rsrc(P.ws, 0, 0x7fffffff, 0x00020000);
    const rsrc_t rc = __builtin_amdgcn_make_buffer_rsrc(P.ctl, 0, CT_BYTES, 0x00020000);
    const unsigned ldsbase = (unsigned)(size_t)lds;
    wait_flags(rc, CT_L, 64, l, 8);
    u32x4 WQ[2][16];
    {
        const char* const mats[1] = {(const char*)P.Wq};
        load_proj_weights<1>(mats, h, l, w, lds, [&](auto, int tb, int kd, u32x4 f) { WQ[tb][kd] = f; });
    }
    bar();
    const XStage xst = make_xstage(P, l, w, ldsbase);
    xst.issue(c, 0);
    xst.issue(c + 2, 1);
    int buf = 0;
    const int T = w & 1, th = w >> 1;
    const int qi = 16 * T + n;
    for (int u = 0; u < 32; ++u) {
        const int s = 2 * u + c;
        if (u == 0) asm volatile("s_waitcnt vmcnt(8)" ::: "memory");
        bar();
        const LAS char* xb = lds + buf * 32768 + l * 16;
        const int bnext = buf == 0 ? 2 : buf - 1;
        f32x4 Q[2][2];
#pragma unroll
        for (int i = 0; i < 2; ++i) { Q[i][0] = (f32x4){0, 0, 0, 0}; Q[i][1] = Q[i][0]; }
        x_stream(xb, [&](int kd, int Tq, const u32x4& xf) { Q[0][Tq] = mf32(WQ[0][kd], xf, Q[0][Tq]); Q[1][Tq] = mf32(WQ[1][kd], xf, Q[1][Tq]); },
                 [&](int i) { if (u < 30) xst.issue1(s + 4, bnext, i); });
        buf = buf == 2 ? 0 : buf + 1;
#pragma unroll
        for (int Tq = 0; Tq < 2; ++Tq)
            *(LAS u32x4*)(lds + L_QL + (4 * Tq + w) * 1024 + l * 16) =
                (u32x4){pack2(Q[0][Tq][0], Q[0][Tq][1]), pack2(Q[0][Tq][2], Q[0][Tq][3]), pack2(Q[1][Tq][0], Q[1][Tq][1]), pack2(Q[1][Tq][2], Q[1][Tq][3])};
        wait_flags(rc, CT_KV + (h * 64 + s) * 4, 1, l, 2);
        const unsigned blk = (s * 32 + h) * 8192 + l * 16;
        u32x4 kx[2][4], vx[4];
#pragma unroll
        for (int kk = 0; kk < 4; ++kk) { kx[0][kk] = ld128(rs, WS_KX + blk + kk * 1024); kx[1][kk] = ld128(rs, WS_KX + blk + (4 + kk) * 1024); }
#pragma unroll
        for (int t = 0; t < 4; ++t) vx[t] = ld128(rs, WS_VX + blk + (4 * th + t) * 1024);
        bar();
        u32x4 qb[4];
#pragma unroll
        for (int kk = 0; kk < 4; ++kk) qb[kk] = lds128(lds + L_QL + (4 * T + kk) * 1024 + l * 16);
        f32x4 S0 = {0, 0, 0, 0}, S1 = S0, S2 = S0;
#pragma unroll
        for (int kk = 0; kk < 4; ++kk) { S0 = mf32(kx[0][kk], qb[kk], S0); S1 = mf32(kx[1][kk], qb[kk], S1); }
        float s0[4], s1[4];
#pragma unroll
        for (int r = 0; r < 4; ++r) { s0[r] = (4 * g + r <= qi) ? S0[r] : -__builtin_inff(); s1[r] = (16 + 4 * g + r <= qi) ? S1[r] : -__builtin_inff(); }
        float m = vmax(vmax3(vmax3(s0[0], s0[1], s0[2]), s0[3], s1[0]), vmax3(s1[1], s1[2], s1[3]));
        wait_flags(rc, CT_M + (s * 64 + 2 * h) * 4, 2, l, 1);
        unsigned vm[4];
        u32x2 kma[4], kmb[4];
#pragma unroll
        for (int kk = 0; kk < 4; ++kk) {
            const unsigned o = WS_KM + (s * 32 + h) * 4096 + kk * 1024 + l * 8;
            kma[kk] = ld64(rs, o); kmb[kk] = ld64(rs, o + 512);
        }
#pragma unroll
        for (int t = 0; t < 4; ++t) vm[t] = ld32(rs, WS_VM + (s * 32 + h) * 2048 + (4 * th + t) * 256 + l * 4);
#pragma unroll
        for (int kk = 0; kk < 4; ++kk) S2 = mf32((u32x4){kma[kk].x, kma[kk].y, kmb[kk].x, kmb[kk].y}, qb[kk], S2);
        m = vmax3(m, S2[0], S2[1]);
        m = xmax32(xmax16(m));
        const float mc = m * CS;
        float p0[4], p1[4], p2[2];
#pragma unroll
        for (int r = 0; r < 4; ++r) { p0[r] = __builtin_amdgcn_exp2f(s0[r] * CS - mc); p1[r] = __builtin_amdgcn_exp2f(s1[r] * CS - mc); }
        p2[0] = __builtin_amdgcn_exp2f(S2[0] * CS - mc); p2[1] = __builtin_amdgcn_exp2f(S2[1] * CS - mc);
        const u32x4 pb = {pack2(p0[0], p0[1]), pack2(p0[2], p0[3]), pack2(p1[0], p1[1]), pack2(p1[2], p1[3])};
        const u32x4 pb2 = {pack2(p2[0], p2[1]), 0u, 0u, 0u};
        float ls = ((p0[0] + p0[1]) + (p0[2] + p0[3])) + ((p1[0] + p1[1]) + (p1[2] + p1[3])) + (p2[0] + p2[1]);
        ls = xadd32(xadd16(ls));
        const float il = __builtin_amdgcn_rcpf(ls);
#pragma unroll
        for (int k2 = 0; k2 < 2; ++k2) {
            f32x4 oa = {0, 0, 0, 0}, ob = oa;
            oa = mf32(vx[2 * k2], pb, oa); ob = mf32(vx[2 * k2 + 1], pb, ob);
            oa = mf32((u32x4){vm[2 * k2], 0u, 0u, 0u}, pb2, oa); ob = mf32((u32x4){vm[2 * k2 + 1], 0u, 0u, 0u}, pb2, ob);
            st128((u32x4){pack2(oa[0] * il, oa[1] * il), pack2(oa[2] * il, oa[3] * il), pack2(ob[0] * il, ob[1] * il), pack2(ob[2] * il, ob[3] * il)}, rs,
                  WS_OX + s * 262144 + (T * 128 + 4 * h + 2 * th + k2) * 1024 + l * 16);
        }
        drain();
        bar();
        if (tid == 0) st32(1u, rc, CT_O + (s * 32 + h) * 4);
    }
}

constexpr int L_RED = 0;
DI void tail_role(const Params& P, const int xg, const int cs, LAS char* lds) {
    const int tid = threadIdx.x, l = tid & 63, w = __builtin_amdgcn_readfirstlane(tid >> 6);
    const int n = l & 15, g = l >> 4;
    const rsrc_t rs = __builtin_amdgcn_make_buffer_rsrc(P.ws, 0, 0x7fffffff, 0x00020000);
    const rsrc_t rc = __builtin_amdgcn_make_buffer_rsrc(P.ctl, 0, CT_BYTES, 0x00020000);
    const unsigned ldsbase = (unsigned)(size_t)lds;
    wait_flags(rc, CT_L, 64, l, 8);
    u32x4 WO[2][32];
    {
        const char* so = (const char*)P.Wo + (size_t)(1024 * w + (l >> 2)) * 1024 + cs * 64 + (((l >> 1) & 1) ^ ((l >> 4) & 1)) * 32 + (l & 1) * 16;
        const int rd = w * 4096 + (4 * g + ((l >> 2) & 3)) * 64 + (l & 3) * 8;
        weight_stream<16, WS_WIN>(lds + L_STG,
            [&](auto cc, int i) -> const char* { constexpr int ch = decltype(cc)::v; return so + (size_t)(64 * ch + 16 * i) * 1024; },
            [&](auto, int i) -> int { return w * 4096 + i * 1024 + l * 16; },
            [&](auto cc, const LAS char* slot) {
                constexpr int ch = decltype(cc)::v;
#pragma unroll
                for (int kdl = 0; kdl < 2; ++kdl)
#pragma unroll
                    for (int tn = 0; tn < 2; ++tn) {
                        const LAS char* a = slot + rd + kdl * 2048 + ((tn ^ (g & 1)) * 32);
                        const u32x2 r0 = trd(a), r1 = trd(a + 1024);
                        WO[tn][2 * ch + kdl] = (u32x4){r0.x, r0.y, r1.x, r1.y};
                    }
            });
    }
    bar();
    for (int s = xg; s < 64; s += 2) {
        wait_flags(rc, CT_O + s * 128, 32, l, 1);
        f32x4 C[2][2];
#pragma unroll
        for (int i = 0; i < 2; ++i) { C[i][0] = (f32x4){0, 0, 0, 0}; C[i][1] = C[i][0]; }
        const unsigned ab = WS_OX + s * 262144 + 32 * w * 1024 + l * 16;
        u32x4 A[2][16];
#pragma unroll
        for (int j = 0; j < 8; ++j) { A[0][2 * j] = ld128(rs, ab + j * 1024); A[0][2 * j + 1] = ld128(rs, ab + (128 + j) * 1024); }
        static_for<0, 4>([&](auto bb) {
            constexpr int b = decltype(bb)::v;
            if constexpr (b < 3) {
#pragma unroll
                for (int j = 0; j < 8; ++j) { A[(b + 1) & 1][2 * j] = ld128(rs, ab + (8 * (b + 1) + j) * 1024); A[(b + 1) & 1][2 * j + 1] = ld128(rs, ab + (128 + 8 * (b + 1) + j) * 1024); }
            }
            SB();
#pragma unroll
            for (int j = 0; j < 8; ++j)
#pragma unroll
                for (int Tq = 0; Tq < 2; ++Tq) { C[Tq][0] = mf32(A[b & 1][2 * j + Tq], WO[0][8 * b + j], C[Tq][0]); C[Tq][1] = mf32(A[b & 1][2 * j + Tq], WO[1][8 * b + j], C[Tq][1]); }
            SB();
        });
        LAS float* red = (LAS float*)(lds + L_RED) + w * (32 * 33);
#pragma unroll
        for (int Tq = 0; Tq < 2; ++Tq)
#pragma unroll
            for (int tn = 0; tn < 2; ++tn)
#pragma unroll
                for (int r = 0; r < 4; ++r) red[(16 * Tq + 4 * g + r) * 33 + 16 * tn + n] = C[Tq][tn][r];
        bar();
        {
            const int tok = tid >> 3, c0 = (tid & 7) * 4;
            const LAS float* rr = (const LAS float*)(lds + L_RED) + tok * 33 + c0;
            float o[4];
#pragma unroll
            for (int j = 0; j < 4; ++j) o[j] = (rr[j] + rr[32 * 33 + j]) + (rr[2 * 32 * 33 + j] + rr[3 * 32 * 33 + j]);
            *(u32x2*)((char*)P.out + ((size_t)(32 * s + tok) * 1024 + 512 + 32 * cs + c0) * 2) = (u32x2){pack2(o[0], o[1]), pack2(o[2], o[3])};
        }
        bar();
    }
}

struct cf { float x, y; };
DI cf cadd(cf a, cf b) { return {a.x + b.x, a.y + b.y}; }
DI cf csub(cf a, cf b) { return {a.x - b.x, a.y - b.y}; }
DI cf cmul(cf a, cf b) { return {a.x * b.x - a.y * b.y, a.x * b.y + a.y * b.x}; }
template <bool INV> DI cf crot(cf a) { return INV ? cf{-a.y, a.x} : cf{a.y, -a.x}; }
template <bool INV> DI void fft4(cf& a0, cf& a1, cf& a2, cf& a3) {
    const cf s02 = cadd(a0, a2), d02 = csub(a0, a2), s13 = cadd(a1, a3), d13 = crot<INV>(csub(a1, a3));
    a0 = cadd(s02, s13); a1 = cadd(d02, d13); a2 = csub(s02, s13); a3 = csub(d02, d13);
}
template <bool INV> DI void fft8(cf (&v)[8]) {
    fft4<INV>(v[0], v[2], v[4], v[6]);
    fft4<INV>(v[1], v[3], v[5], v[7]);
    constexpr float h = 0.70710678118654752f;
    const cf o0 = v[1];
    const cf r1 = crot<INV>(v[3]); const cf o1 = {h * (v[3].x + r1.x), h * (v[3].y + r1.y)};
    const cf o2 = crot<INV>(v[5]);
    const cf r3 = crot<INV>(v[7]); const cf o3 = {h * (r3.x - v[7].x), h * (r3.y - v[7].y)};
    const cf e0 = v[0], e1 = v[2], e2 = v[4], e3 = v[6];
    v[0] = cadd(e0, o0); v[1] = cadd(e1, o1); v[2] = cadd(e2, o2); v[3] = cadd(e3, o3);
    v[4] = csub(e0, o0); v[5] = csub(e1, o1); v[6] = csub(e2, o2); v[7] = csub(e3, o3);
}
DI int fpad(int i) { return i + (i >> 3); }
DI cf ldc(const LAS char* b, int i) { const f32x2 v = *(const LAS f32x2*)(b + i * 8); return {v[0], v[1]}; }
DI void stc(LAS char* b, int i, cf v) { *(LAS f32x2*)(b + i * 8) = (f32x2){v.x, v.y}; }
template <bool INV> DI cf twid(const LAS char* tw, int i) { cf t = ldc(tw, i); if (INV) t.y = -t.y; return t; }
template <bool INV, int NS> DI void pass8(cf (&v)[8], int t, const LAS char* tw, LAS char* dst) {
    const int k = t & (NS - 1);
    if (NS > 1) {
#pragma unroll
        for (int r = 1; r < 8; ++r) v[r] = cmul(v[r], twid<INV>(tw, k * r * (256 / NS)));
    }
    fft8<INV>(v);
    const int j = (t - k) * 8 + k;
#pragma unroll
    for (int r = 0; r < 8; ++r) stc(dst, fpad(j + r * NS), v[r]);
}
template <int STR> DI void gather8(cf (&v)[8], int t, const LAS char* src) {
#pragma unroll
    for (int r = 0; r < 8; ++r) v[r] = ldc(src, fpad(t + r * STR));
}
DI void wsync() { asm volatile("s_waitcnt lgkmcnt(0)" ::: "memory"); }
template <bool INV> DI void fft512(cf (&v)[8], int l, const LAS char* tw, LAS char* buf) {
    pass8<INV, 1>(v, l, tw, buf); wsync(); gather8<64>(v, l, buf); wsync();
    pass8<INV, 8>(v, l, tw, buf); wsync(); gather8<64>(v, l, buf); wsync();
#pragma unroll
    for (int r = 1; r < 8; ++r) v[r] = cmul(v[r], twid<INV>(tw, l * r * 4));
    fft8<INV>(v);
}
template <bool INV> DI void fft2048(cf (&v)[8], int t, const LAS char* tw, LAS char* b0, LAS char* b1) {
    pass8<INV, 1>(v, t, tw, b0); bar(); gather8<256>(v, t, b0);
    pass8<INV, 8>(v, t, tw, b1); bar(); gather8<256>(v, t, b1);
    pass8<INV, 64>(v, t, tw, b0); bar(); gather8<256>(v, t, b0);
#pragma unroll
    for (int u = 0; u < 2; ++u) {
        const int k = t + 256 * u;
#pragma unroll
        for (int r = 1; r < 4; ++r) v[u + 2 * r] = cmul(v[u + 2 * r], twid<INV>(tw, k * r));
        fft4<INV>(v[u], v[u + 2], v[u + 4], v[u + 6]);
    }
    bar();
}
constexpr int LF_TW = 0, LF_T = 16384, LF_W = 84352, LF_B0 = 16384, LF_B1 = 16384 + 18432;
DI float bf2f(unsigned short x) { return __builtin_bit_cast(float, (unsigned)x << 16); }
DI void fft_role(const Params& P, const int j, LAS char* lds) {
    const int tid = threadIdx.x, l = tid & 63, w = __builtin_amdgcn_readfirstlane(tid >> 6);
    const rsrc_t rs = __builtin_amdgcn_make_buffer_rsrc(P.ws, 0, 0x7fffffff, 0x00020000);
    const rsrc_t rc = __builtin_amdgcn_make_buffer_rsrc(P.ctl, 0, CT_BYTES, 0x00020000);
    const LAS char* tw = lds + LF_TW;
    for (int i = tid; i < 2048; i += 256) { float sn, cn; sincospif((float)i * (1.0f / 1024.0f), &sn, &cn); stc(lds + LF_TW, i, cf{cn, -sn}); }
    bar();
    const int s0 = 32 * j;
    LAS char* T = lds + LF_T;
    LAS char* wb = lds + LF_W + w * 4608;
    for (int pi = 4 * w; pi < 4 * w + 4; ++pi) {
        const unsigned short* xa = (const unsigned short*)P.Ax + (size_t)(s0 + 2 * pi) * 512;
        cf v[8];
#pragma unroll
        for (int r = 0; r < 8; ++r) v[r] = cf{bf2f(xa[l + 64 * r]), bf2f(xa[512 + l + 64 * r])};
        fft512<false>(v, l, tw, wb);
#pragma unroll
        for (int r = 0; r < 8; ++r) stc(wb, fpad(l + 64 * r), v[r]);
        wsync();
#pragma unroll
        for (int r = 0; r < 4; ++r) {
            const int k = l + 64 * r;
            const cf a = ldc(wb, fpad(k)), b = ldc(wb, fpad((512 - k) & 511));
            stc(T, k * 33 + 2 * pi, cf{0.5f * (a.x + b.x), 0.5f * (a.y - b.y)});
            stc(T, k * 33 + 2 * pi + 1, cf{0.5f * (a.y + b.y), 0.5f * (b.x - a.x)});
        }
        if (l == 0) { const cf a = ldc(wb, fpad(256)); stc(T, 256 * 33 + 2 * pi, cf{a.x, 0.f}); stc(T, 256 * 33 + 2 * pi + 1, cf{a.y, 0.f}); }
        wsync();
    }
    bar();
    for (int k = tid >> 4; k < 257; k += 16) {
        const int c = tid & 15;
        const cf a = ldc(T, k * 33 + 2 * c), b = ldc(T, k * 33 + 2 * c + 1);
        st128((u32x4){f2u(a.x), f2u(a.y), f2u(b.x), f2u(b.y)}, rs, WS_F + (k * 2048 + s0 + 2 * c) * 8);
    }
    drain();
    bar();
    if (tid == 0) st32(1u, rc, CT_F1 + j * 4);
    wait_flags(rc, CT_F1, 64, l, 4);
    const int ncol = (j == 0) ? 5 : 4;
    for (int ci = 0; ci < ncol; ++ci) {
        const int k = (ci < 4) ? 4 * j + ci : 256;
        cf v[8];
#pragma unroll
        for (int r = 0; r < 8; ++r) { const u32x2 x = ld64(rs, WS_F + (k * 2048 + tid + 256 * r) * 8); v[r] = cf{u2f(x.x), u2f(x.y)}; }
        cf W[8];
#pragma unroll
        for (int r = 0; r < 8; ++r) { const size_t o = (size_t)(tid + 256 * r) * 257 + k; W[r] = cf{bf2f(((const unsigned short*)P.wr)[o]), bf2f(((const unsigned short*)P.wi)[o])}; }
        fft2048<false>(v, tid, tw, lds + LF_B0, lds + LF_B1);
#pragma unroll
        for (int r = 0; r < 8; ++r) v[r] = cmul(v[r], W[r]);
        fft2048<true>(v, tid, tw, lds + LF_B0, lds + LF_B1);
#pragma unroll
        for (int r = 0; r < 8; ++r) st64((u32x2){f2u(v[r].x), f2u(v[r].y)}, rs, WS_F + (k * 2048 + tid + 256 * r) * 8);
    }
    drain();
    bar();
    if (tid == 0) st32(1u, rc, CT_F2 + j * 4);
    wait_flags(rc, CT_F2, 64, l, 4);
    for (int k = tid >> 4; k < 257; k += 16) {
        const int c = tid & 15;
        const u32x4 x = ld128(rs, WS_F + (k * 2048 + s0 + 2 * c) * 8);
        stc(T, k * 33 + 2 * c, cf{u2f(x.x), u2f(x.y)}); stc(T, k * 33 + 2 * c + 1, cf{u2f(x.z), u2f(x.w)});
    }
    bar();
    for (int pi = 4 * w; pi < 4 * w + 4; ++pi) {
        cf v[8];
#pragma unroll
        for (int r = 0; r < 8; ++r) {
            const int kk = l + 64 * r, k = kk <= 256 ? kk : 512 - kk;
            const cf a = ldc(T, k * 33 + 2 * pi), b = ldc(T, k * 33 + 2 * pi + 1);
            const bool edge = (k == 0) | (k == 256);
            const float ay = edge ? 0.f : a.y, by = edge ? 0.f : b.y;
            v[r] = kk <= 256 ? cf{a.x - by, ay + b.x} : cf{a.x + by, b.x - ay};
        }
        fft512<true>(v, l, tw, wb);
        unsigned short* o = (unsigned short*)P.out + (size_t)(s0 + 2 * pi) * 1024;
        constexpr float sc = 1.0f / (2048.0f * 512.0f);
#pragma unroll
        for (int r = 0; r < 8; ++r) {
            const unsigned pk = pack2(v[r].x * sc, v[r].y * sc);
            o[l + 64 * r] = (unsigned short)pk; o[1024 + l + 64 * r] = (unsigned short)(pk >> 16);
        }
        wsync();
    }
}

__global__ void __launch_bounds__(256) gfnet_rmt_init(char* ctl, char* ws) {
    const int tid = threadIdx.x, b = blockIdx.x;
    if (b == 2) {
#pragma unroll
        for (int i = 0; i < (int)(CT_BYTES / 4096); ++i)
            if (i * 4096 + tid * 16 < (int)CT_CFG) *(u32x4*)(ctl + i * 4096 + tid * 16) = (u32x4){0, 0, 0, 0};
            else if (i * 4096 + tid * 16 >= (int)CT_TK) *(u32x4*)(ctl + i * 4096 + tid * 16) = (u32x4){0, 0, 0, 0};
        return;
    }
    if (tid != 0) return;
    unsigned best[2] = {~0u, ~0u};
#pragma unroll
    for (int r = 0; r < 6; ++r) {
        unsigned* p = (unsigned*)(ws + b * (WS_RB - WS_RA) + 70 * 16384 + (r & 1) * 8192);
        unsigned v = 0; unsigned long long t0, t1;
        asm volatile("s_waitcnt vmcnt(0)\n\ts_memtime %1\n\ts_waitcnt lgkmcnt(0)\n\tglobal_atomic_or %0, %3, %0, off sc0\n\ts_waitcnt vmcnt(0)\n\ts_memtime %2\n\ts_waitcnt lgkmcnt(0)"
                     : "+&v"(v), "=&s"(t0), "=&s"(t1) : "v"(p) : "memory");
        const unsigned d = (unsigned)(t1 - t0);
        best[r & 1] = d < best[r & 1] ? d : best[r & 1];
    }
    const unsigned xcc = (unsigned)__builtin_amdgcn_s_getreg(20 | (3 << 11)) & 7u;
    *(unsigned*)(ctl + CT_CFG + b * 4) = ((best[1] < best[0]) ^ (xcc >> 2)) ? 8192u : 0u;
}

__global__ void __launch_bounds__(256, 1) gfnet_rmt_kernel(Params P) {
    extern __shared__ __attribute__((aligned(16))) char smem[];
    LAS char* lds = (LAS char*)smem;
    int x, ix;
    {
        const rsrc_t rc = __builtin_amdgcn_make_buffer_rsrc(P.ctl, 0, CT_BYTES, 0x00020000);
        LAS unsigned* sh = (LAS unsigned*)lds;
        if (threadIdx.x == 0) {
            const unsigned xcc = (unsigned)__builtin_amdgcn_s_getreg(20 | (3 << 11)) & 7u;
            unsigned* tk = (unsigned*)(P.ctl + CT_TK);
            unsigned xx = xcc, t = __hip_atomic_fetch_add(tk + xcc * 32, 1u, __ATOMIC_RELAXED, __HIP_MEMORY_SCOPE_AGENT);
            if (t >= 32) {
                unsigned o = __hip_atomic_fetch_add(tk + 8 * 32, 1u, __ATOMIC_RELAXED, __HIP_MEMORY_SCOPE_AGENT);
                unsigned cnt[8];
                for (int spins = 0; spins < SPIN_CAP; ++spins) {
                    unsigned tot = 0;
                    for (int i = 0; i < 8; ++i) { cnt[i] = ld32(rc, CT_TK + i * 128); tot += cnt[i]; }
                    if (tot == 256) break;
                    __builtin_amdgcn_s_sleep(4);
                }
                xx = 0; t = 0;
                for (int i = 0; i < 8; ++i) { const unsigned fr = cnt[i] < 32 ? 32 - cnt[i] : 0; if (o < fr) { xx = i; t = cnt[i] + o; break; } o -= fr; }
            }
            sh[0] = xx; sh[1] = t;
        }
        __syncthreads();
        x = __builtin_amdgcn_readfirstlane(sh[0]); ix = __builtin_amdgcn_readfirstlane(sh[1]);
        __syncthreads();
    }
    if (x < 4) {
        if (ix < 16) { const int r = x * 16 + ix; chain_role(P, r >> 1, r & 1, lds); }
        else fft_role(P, x * 16 + ix - 16, lds);
    } else {
        if (ix < 8) kv_role(P, (x - 4) * 8 + ix, lds);
        else if (ix < 24) qa_role(P, (x - 4) * 8 + ((ix - 8) >> 1), ix & 1, lds);
        else tail_role(P, (x - 4) >> 1, ((x - 4) & 1) * 8 + ix - 24, lds);
    }
}

#define DBG_D1 5
#define DBG_D2 10
extern "C" void kernel_launch(void* const* d_in, const int* in_sizes, int n_in, void* d_out, int out_size, void* d_ws, size_t ws_size,
                              hipStream_t stream) {
    (void)in_sizes; (void)n_in; (void)out_size; (void)ws_size;
    Params P;
    P.Ax = d_in[0]; P.wr = d_in[1]; P.wi = d_in[2]; P.Bx = d_in[3]; P.mem0 = d_in[4];
    P.Wq = d_in[5]; P.Wk = d_in[6]; P.Wv = d_in[7]; P.Wo = d_in[8];
    P.out = d_out; P.ctl = (char*)d_ws;
    P.ws = (char*)(((uintptr_t)d_ws + CT_BYTES + (2u << 20) - 1) & ~(uintptr_t)((2u << 20) - 1));
    P.d1 = DBG_D1; P.d2 = DBG_D2;
    (void)hipFuncSetAttribute((const void*)gfnet_rmt_kernel, hipFuncAttributeMaxDynamicSharedMemorySize, LDS_BYTES);
    gfnet_rmt_init<<<3, 256, 0, stream>>>(P.ctl, P.ws);
    gfnet_rmt_kernel<<<256, 256, LDS_BYTES, stream>>>(P);
}
```
